```python
import torch

def _softmax(x, axis=-1):
    return torch.softmax(x, dim=axis)
def _gelu(x):
    return torch.nn.functional.gelu(x, approximate="tanh")
def _silu(x):
    return torch.nn.functional.silu(x)
def _sigmoid(x):
    return torch.sigmoid(x)
def _layernorm(x, axis=-1, eps=1e-5):
    m = x.mean(dim=axis, keepdim=True)
    v = x.var(dim=axis, keepdim=True, unbiased=False)
    return (x - m) / torch.sqrt(v + eps)
def _scatter_add(tgt, idx, src, axis=0):
    return tgt.clone().index_add_(axis, idx, src)
def _segment_sum(src, seg, n):
    out = torch.zeros((n,) + src.shape[1:], dtype=src.dtype)
    return out.index_add_(0, seg, src)
def _pad(x, pw):
    flat = tuple(v for pair in reversed(pw) for v in pair)
    return torch.nn.functional.pad(x, flat)

kernel_name = 'sequential_preLN__linear_attn_block__dense_activation_mish__prompt'

def setup_inputs():

    _w = (lambda a: (lambda t: t.to(torch.bfloat16) if t.is_floating_point() else t)(torch.as_tensor(a)))
    def _A_setup():
        B, S, D = 1, 2048, 512
        return {
            "x": _w((lambda *s: torch.randn(*s, dtype=torch.bfloat16) * 0.05)(B, S, D)),
            "wqkv": _w((lambda *s: torch.randn(*s, dtype=torch.bfloat16) * 0.05)(D, 3 * D)),
            "wo": _w((lambda *s: torch.randn(*s, dtype=torch.bfloat16) * 0.05)(D, D)),
        }
    _A_in = _A_setup()

    def _B_setup():
        B, S, D, F = 1, 2048, 512, 1376
        return {
            "x": _w((lambda *s: torch.randn(*s, dtype=torch.bfloat16) * 0.05)(B, S, D)),
            "w1": _w((lambda *s: torch.randn(*s, dtype=torch.bfloat16) * 0.8)(D, F) * (D ** -0.5)),
            "b1": _w((lambda *s: torch.randn(*s, dtype=torch.bfloat16) * 0.2)(F)),
            "w2": _w((lambda *s: torch.randn(*s, dtype=torch.bfloat16) * 0.1)(F, D) * (F ** -0.5)),
        }
    _B_in = _B_setup()

    return {
        "A_x": _A_in["x"],
        "A_wqkv": _A_in["wqkv"],
        "A_wo": _A_in["wo"],
        "B_x": _B_in["x"],
        "B_w1": _B_in["w1"],
        "B_b1": _B_in["b1"],
        "B_w2": _B_in["w2"],
    }

def reference(A_x, A_wqkv, A_wo, B_x, B_w1, B_b1, B_w2):
    def _A_ref(x, wqkv, wo):
        B, S, D = x.shape
        H = 32; Dh = D // H
        qkv = (lambda x, *d: x.permute(*d))((x @ wqkv).reshape(B, S, 3, H, Dh), 2, 0, 3, 1, 4)
        q, k, v = qkv[0], qkv[1], qkv[2]
        phi = lambda t: torch.where(t > 0, t + 1, torch.exp(t)) + 1e-6
        pq, pk = phi(q), phi(k)
        kv = torch.einsum("bhsd,bhse->bhde", pk, v)
        z = torch.einsum("bhsd->bhd", pk)
        num = torch.einsum("bhqd,bhde->bhqe", pq, kv)
        den = torch.einsum("bhqd,bhd->bhq", pq, z)[..., None] + 1e-6
        o = num / den
        return (lambda x, *d: x.permute(*d))(o, 0, 2, 1, 3).reshape(B, S, D) @ wo

    def _B_ref(x, w1, b1, w2):
        h = torch.einsum("bsd,df->bsf", x, w1) + b1
        sp = torch.log1p(torch.exp(-torch.abs(h))) + torch.where(h > 0, h, torch.zeros_like(h))
        act = h * torch.tanh(sp)
        return torch.einsum("bsf,fd->bsd", act, w2)

    x = A_x
    h = x + _A_ref(_layernorm(x, axis=-1), A_wqkv, A_wo)
    h2 = h + _B_ref(_layernorm(h, axis=-1), B_w1, B_b1, B_w2)
    return h2
```

```cpp
#include <hip/hip_runtime.h>
#include <hip/hip_bf16.h>

constexpr int S = 2048, D = 512, F = 1376, H = 32, DH = 16;
typedef unsigned short u16;
__device__ __forceinline__ float bf2f(u16 b) { return __uint_as_float(((unsigned)b) << 16); }
__device__ __forceinline__ u16 f2bf(float f) { __hip_bfloat16 h = __float2bfloat16(f); return *(u16*)&h; }

template <typename T> __global__ void ln_k(const T* in, float* out) {
  int r = blockIdx.x; int t = threadIdx.x;
  float v[8]; float s = 0;
  for (int i = 0; i < 8; i++) { float x; if constexpr (sizeof(T) == 2) x = bf2f(in[r * D + t * 8 + i]); else x = in[r * D + t * 8 + i]; v[i] = x; s += x; }
  for (int m = 32; m; m >>= 1) s += __shfl_xor(s, m);
  float mean = s / D; float q = 0;
  for (int i = 0; i < 8; i++) q += (v[i] - mean) * (v[i] - mean);
  for (int m = 32; m; m >>= 1) q += __shfl_xor(q, m);
  float rs = 1.0f / sqrtf(q / D + 1e-5f);
  for (int i = 0; i < 8; i++) out[r * D + t * 8 + i] = (v[i] - mean) * rs;
}
__global__ void gemm_k(const float* A, const u16* B, float* C, int M, int N, int K) {
  __shared__ float sa[16][17], sb[16][17];
  int tx = threadIdx.x & 15, ty = threadIdx.x >> 4;
  int row = blockIdx.y * 16 + ty, col = blockIdx.x * 16 + tx;
  float acc = 0;
  for (int k0 = 0; k0 < K; k0 += 16) {
    sa[ty][tx] = A[row * K + k0 + tx];
    sb[ty][tx] = bf2f(B[(k0 + ty) * N + col]);
    __syncthreads();
    for (int k = 0; k < 16; k++) acc += sa[ty][k] * sb[k][tx];
    __syncthreads();
  }
  C[row * N + col] = acc;
}
__device__ __forceinline__ float phi(float t) { return (t > 0 ? t + 1.f : expf(t)) + 1e-6f; }
__global__ void kv_k(const float* qkv, float* kv, float* z) {
  int h = blockIdx.x; int d = threadIdx.x >> 4, e = threadIdx.x & 15;
  float acc = 0, zz = 0;
  for (int s = 0; s < S; s++) { float pk = phi(qkv[s * 1536 + 512 + h * 16 + d]); acc += pk * qkv[s * 1536 + 1024 + h * 16 + e]; zz += pk; }
  kv[h * 256 + d * 16 + e] = acc; if (e == 0) z[h * 16 + d] = zz;
}
__global__ void o_k(const float* qkv, const float* kv, const float* z, float* o) {
  int s = blockIdx.x; int c = threadIdx.x; int h = c >> 4, e = c & 15;
  float num = 0, den = 0;
  for (int d = 0; d < 16; d++) { float pq = phi(qkv[s * 1536 + h * 16 + d]); num += pq * kv[h * 256 + d * 16 + e]; den += pq * z[h * 16 + d]; }
  o[s * D + c] = num / (den + 1e-6f);
}
__global__ void addx_k(const u16* x, float* h) { int i = blockIdx.x * 256 + threadIdx.x; h[i] += bf2f(x[i]); }
__global__ void mish_k(float* f, const u16* b1) {
  int i = blockIdx.x * 256 + threadIdx.x; float v = f[i] + bf2f(b1[i % F]);
  float sp = log1pf(expf(-fabsf(v))) + (v > 0 ? v : 0.f);
  f[i] = v * tanhf(sp);
}
__global__ void fin_k(const float* h, const float* y, u16* out) { int i = blockIdx.x * 256 + threadIdx.x; out[i] = f2bf(h[i] + y[i]); }

extern "C" void kernel_launch(void* const* d_in, const int* in_sizes, int n_in, void* d_out, int out_size, void* d_ws, size_t ws_size, hipStream_t stream) {
  const u16* x = (const u16*)d_in[0]; const u16* wqkv = (const u16*)d_in[1]; const u16* wo = (const u16*)d_in[2];
  const u16* w1 = (const u16*)d_in[4]; const u16* b1 = (const u16*)d_in[5]; const u16* w2 = (const u16*)d_in[6];
  float* ws = (float*)d_ws;
  float* xn = ws; float* qkv = xn + S * D; float* kv = qkv + S * 1536; float* z = kv + 32 * 256; float* o = z + 512;
  float* h = o + S * D; float* hn = h + S * D; float* f = hn + S * D; float* y = f + S * F;
  ln_k<u16><<<S, 64, 0, stream>>>(x, xn);
  gemm_k<<<dim3(1536 / 16, S / 16), 256, 0, stream>>>(xn, wqkv, qkv, S, 1536, D);
  kv_k<<<32, 256, 0, stream>>>(qkv, kv, z);
  o_k<<<S, 512, 0, stream>>>(qkv, kv, z, o);
  gemm_k<<<dim3(D / 16, S / 16), 256, 0, stream>>>(o, wo, h, S, D, D);
  addx_k<<<S * D / 256, 256, 0, stream>>>(x, h);
  ln_k<float><<<S, 64, 0, stream>>>(h, hn);
  gemm_k<<<dim3(F / 16, S / 16), 256, 0, stream>>>(hn, w1, f, S, F, D);
  mish_k<<<S * F / 256, 256, 0, stream>>>(f, b1);
  gemm_k<<<dim3(D / 16, S / 16), 256, 0, stream>>>(f, w2, y, S, D, F);
  fin_k<<<S * D / 256, 256, 0, stream>>>(h, y, (u16*)d_out);
}
```

```cpp
#include <hip/hip_runtime.h>

constexpr unsigned WS_KVP = 262144u, KVP_SZ = 2304u, WS_O = 16777216u, WS_H = 20971520u, WS_ST = 5242880u, WS_ACT = 25165824u, WS_STAMP = 12582912u;
constexpr unsigned LDS_BYTES = 163840;
#define FRAG32 1

__global__ __launch_bounds__(256, 1) void fused_block(const void* x, const void* wqkv, const void* wo, const void* w1, const void* b1,
                                                      const void* w2, void* out, void* ws) {
  unsigned tid = threadIdx.x;
  asm volatile(
    "s_load_dwordx16 s[8:23], %1, 0\n"
    "s_mov_b32 s56, %2\n"
    "v_lshrrev_b32 v61, 6, v0\n"
    "v_and_b32 v23, 63, v0\n"
    "v_readfirstlane_b32 s59, v61\n"
    "s_getpc_b64 s[70:71]\n"
    ".Lpc_%=:\n"
    "s_lshr_b32 s75, s56, 3\n"
    "s_lshl_b32 s75, s75, 2\n"
    "s_add_u32 s75, s75, s59\n"
    "s_lshl_b32 s75, s75, 7\n"
    "v_and_b32 v62, 3, v23\n"
    "v_lshl_add_u32 v62, v62, 14, s75\n"
    "v_min_u32 v62, .Lmend_%=-.Lpc_%=-4, v62\n"
    "global_load_dword v253, v62, s[70:71]\n"
    "s_bfe_u32 s57, s56, 0x20001\n"
    "s_lshl_b32 s57, s57, 2\n"
    "s_bfe_u32 s75, s56, 0x20003\n"
    "s_or_b32 s57, s57, s75\n"
    "s_and_b32 s60, s56, 1\n"
    "s_lshr_b32 s61, s56, 5\n"
    "s_lshl_b32 s58, s60, 3\n"
    "s_or_b32 s58, s58, s61\n"
    "v_and_b32 v21, 15, v23\n"
    "v_lshrrev_b32 v22, 4, v23\n"
    "v_lshlrev_b32 v1, 4, v23\n"
    "v_bfe_u32 v64, v23, 2, 2\n"
    "v_and_b32 v65, 3, v23\n"
    "v_lshrrev_b32 v66, 3, v23\n"
    "v_and_b32 v67, 7, v23\n"
    "v_and_b32 v68, 1, v22\n"
    "v_lshlrev_b32 v61, 1, v68\n"
    "v_xor_b32 v61, v61, v65\n"
    "v_lshlrev_b32 v61, 4, v61\n"
    "v_lshl_add_u32 v62, v22, 3, v64\n"
    "v_mul_u32_u24 v62, 0xc00, v62\n"
    "v_add_u32 v15, v62, v61\n"
    "v_lshrrev_b32 v61, 1, v66\n"
    "v_xor_b32 v62, v61, v67\n"
    "v_lshlrev_b32 v62, 4, v62\n"
    "v_lshl_add_u32 v16, v66, 10, v62\n"
    "v_xor_b32 v17, 64, v16\n"
    "v_lshlrev_b32 v61, 1, v61\n"
    "v_xor_b32 v62, v61, v67\n"
    "v_lshlrev_b32 v62, 4, v62\n"
    "v_lshl_add_u32 v14, v66, 10, v62\n"
    "s_lshl_b32 s75, s57, 7\n"
    "s_lshl_b32 s76, s58, 3\n"
    "s_add_u32 s96, s75, s76\n"
    "s_lshl_b32 s76, s59, 1\n"
    "s_add_u32 s96, s96, s76\n"
    "s_lshl_b32 s76, s59, 5\n"
    "s_add_u32 s75, s75, s76\n"
    "s_lshl_b32 s80, s75, 10\n"
    "s_mul_i32 s81, s59, 0x6000\n"
    "s_add_u32 s81, s81, 0x10000\n"
    "s_lshr_b32 s75, s59, 1\n"
    "s_and_b32 s76, s59, 1\n"
    "s_mul_i32 s82, s75, 0x18000\n"
    "s_lshl_b32 s77, s58, 6\n"
    "s_add_u32 s82, s82, s77\n"
    "s_mul_i32 s77, s76, 0x3000\n"
    "s_add_u32 s84, s82, s77\n"
    "s_lshl_b32 s77, s76, 10\n"
    "s_add_u32 s82, s82, s77\n"
    "s_add_u32 s82, s82, 0x400\n"
    "s_lshl_b32 s83, s75, 12\n"
    "s_lshl_b32 s77, s76, 11\n"
    "s_add_u32 s83, s83, s77\n"
    "s_lshl_b32 s97, s57, 1\n"
    "s_add_u32 s97, s97, s58\n"
    "s_mov_b32 s26, 0x200000\n"
    "s_mov_b32 s27, 0x20000\n"
    "s_mov_b32 s30, 0x180000\n"
    "s_mov_b32 s31, 0x20000\n"
    "s_mov_b32 s34, 0x80000\n"
    "s_mov_b32 s35, 0x20000\n"
    "s_mov_b32 s38, 0x158000\n"
    "s_mov_b32 s39, 0x20000\n"
    "s_mov_b32 s42, 0xac0\n"
    "s_mov_b32 s43, 0x20000\n"
    "s_mov_b32 s46, 0x158000\n"
    "s_mov_b32 s47, 0x20000\n"
    "s_mov_b32 s50, 0x200000\n"
    "s_mov_b32 s51, 0x20000\n"
    "s_mov_b32 s54, 0x2800000\n"
    "s_mov_b32 s55, 0x20000\n"
    "s_waitcnt lgkmcnt(0)\n"
    "s_mov_b32 s28, s10\n"
    "s_and_b32 s29, s11, 0xffff\n"
    "s_mov_b32 s24, s8\n"
    "s_and_b32 s25, s9, 0xffff\n"
    "s_add_u32 s77, s97, 0\n"
    "s_and_b32 s77, s77, 7\n"
    "s_mul_i32 s87, s77, 0x30000\n"
    "s_add_u32 s87, s87, s82\n"
    "s_add_u32 m0, s83, 0\n"
    "s_nop 0\n"
    "buffer_load_dwordx4 v15, s[28:31], s87 offen lds\n"
    "s_add_u32 s77, s97, 0\n"
    "s_and_b32 s77, s77, 7\n"
    "s_lshl_b32 s86, s77, 7\n"
    "s_add_u32 s86, s86, s80\n"
    "s_add_u32 m0, s81, 0\n"
    "s_nop 0\n"
    "buffer_load_dwordx4 v16, s[24:27], s86 offen lds\n"
    "s_add_u32 s66, s86, 0x1c00\n"
    "buffer_load_dwordx4 v17, s[24:27], s66 offen offset:1024 lds\n"
    "s_add_u32 m0, s83, 0x400\n"
    "s_add_u32 s65, s87, 0x3000\n"
    "buffer_load_dwordx4 v15, s[28:31], s65 offen lds\n"
    "s_add_u32 m0, s81, 0x800\n"
    "s_addk_i32 s66, 9216\n"
    "buffer_load_dwordx4 v16, s[24:27], s66 offen lds\n"
    "s_addk_i32 s66, 7168\n"
    "buffer_load_dwordx4 v17, s[24:27], s66 offen offset:1024 lds\n"
    "s_mov_b32 s52, s22\n"
    "s_and_b32 s53, s23, 0xffff\n"
    "buffer_load_dword v60, off, s[52:55], 0 sc1\n"
    "s_lshl_b32 s75, s96, 10\n"
    "buffer_load_dwordx4 v[196:199], v1, s[24:27], s75 offen\n"
    "buffer_load_dwordx4 v[200:203], v1, s[24:27], s75 offen offset:1024\n"
    "s_mov_b32 s32, s12\n"
    "s_and_b32 s33, s13, 0xffff\n"
    "s_mov_b32 s36, s14\n"
    "s_and_b32 s37, s15, 0xffff\n"
    "s_mov_b32 s40, s16\n"
    "s_and_b32 s41, s17, 0xffff\n"
    "s_mov_b32 s44, s18\n"
    "s_and_b32 s45, s19, 0xffff\n"
    "s_mov_b32 s48, s20\n"
    "s_and_b32 s49, s21, 0xffff\n"
    "v_lshlrev_b32 v61, 8, v22\n"
    "v_lshl_add_u32 v61, v64, 6, v61\n"
    "v_lshl_add_u32 v61, v65, 3, v61\n"
    "v_lshl_add_u32 v2, v68, 5, v61\n"
    "v_xor_b32 v3, 32, v2\n"
    "v_add_u32 v4, 0xc000, v2\n"
    "v_add_u32 v5, 0xc000, v3\n"
    "v_add_u32 v246, 0x18000, v2\n"
    "v_add_u32 v247, 0x18000, v3\n"
    "v_lshl_add_u32 v61, v22, 2, v64\n"
    "v_lshlrev_b32 v61, 7, v61\n"
    "v_lshl_add_u32 v61, v65, 3, v61\n"
    "v_lshrrev_b32 v62, 1, v64\n"
    "v_lshl_add_u32 v62, v68, 1, v62\n"
    "v_lshl_add_u32 v61, v62, 5, v61\n"
    "v_add_u32 v6, 0x18000, v61\n"
    "v_xor_b32 v7, 32, v6\n"
    "v_xor_b32 v8, 64, v6\n"
    "v_xor_b32 v9, 0x60, v6\n"
    "v_lshrrev_b32 v61, 1, v21\n"
    "v_xor_b32 v61, v61, v22\n"
    "v_lshlrev_b32 v61, 4, v61\n"
    "v_lshl_add_u32 v61, v21, 7, v61\n"
    "v_add_u32 v18, s81, v61\n"
    "v_xor_b32 v19, 64, v18\n"
    "v_mov_b32 v24, 0x3f803f80\n"
    "v_mov_b32 v25, 0x3f803f80\n"
    "v_mov_b32 v26, 0x3f803f80\n"
    "v_mov_b32 v27, 0x3f803f80\n"
    "s_bfm_b32 s75, s58, 0\n"
    "s_and_b32 s75, s75, 0x88c8\n"
    "s_bcnt1_i32_b32 s75, s75\n"
    "s_mul_i32 s64, s58, 3\n"
    "s_sub_u32 s64, s64, s75\n"
    "s_lshr_b32 s75, 0x88c8, s58\n"
    "s_and_b32 s75, s75, 1\n"
    "s_sub_u32 s63, 3, s75\n"
    "v_lshrrev_b32 v63, 5, v23\n"
    "v_lshlrev_b32 v63, 4, v63\n"
    "v_bfe_u32 v61, v23, 3, 2\n"
    "v_add_u32 v62, v63, v61\n"
    "v_mul_u32_u24 v20, 0xac0, v62\n"
    "v_lshl_add_u32 v20, v65, 4, v20\n"
    "v_add_u32 v61, s64, v66\n"
    "v_and_b32 v61, 1, v61\n"
    "v_lshlrev_b32 v61, 7, v61\n"
    "v_xor_b32 v62, v64, v22\n"
    "v_and_b32 v62, 1, v62\n"
    "v_cmp_eq_u32 vcc, 0, v62\n"
    "v_mov_b32 v62, 64\n"
    "s_nop 0\n"
    "v_cndmask_b32 v62, v61, v62, vcc\n"
    "v_add_u32 v20, v20, v62\n"
    "v_xor_b32 v233, 32, v20\n"
    "v_lshl_add_u32 v63, v68, 2, v63\n"
    "v_and_b32 v62, 1, v64\n"
    "v_lshl_add_u32 v63, v62, 1, v63\n"
    "v_and_b32 v62, 1, v66\n"
    "v_add_u32 v63, v63, v62\n"
    "v_mul_u32_u24 v234, 0xac0, v63\n"
    "v_xor_b32 v61, 0x80, v61\n"
    "v_add_u32 v234, v234, v61\n"
    "v_lshlrev_b32 v62, 1, v68\n"
    "v_xor_b32 v62, v62, v65\n"
    "v_lshl_add_u32 v234, v62, 4, v234\n"
    "s_cmp_eq_u32 s63, 2\n"
    "s_cselect_b32 s75, 0xfffe00, 0\n"
    "v_and_b32 v62, 0x80, v61\n"
    "v_mul_u32_u24 v62, s75, v62\n"
    "v_add_u32 v234, v234, v62\n"
    "v_xor_b32 v61, 0x80, v61\n"
    "v_mov_b32 v62, 0\n"
    "v_cndmask_b32 v62, v61, v62, vcc\n"
    "v_mul_u32_u24 v62, s75, v62\n"
    "v_add_u32 v20, v20, v62\n"
    "v_add_u32 v233, v233, v62\n"
    "s_and_b32 s76, s59, 1\n"
    "s_lshl_b32 s76, s76, 5\n"
    "s_lshr_b32 s75, s59, 1\n"
    "s_lshl_b32 s75, s75, 11\n"
    "v_xor_b32 v232, s76, v2\n"
    "v_add_u32 v232, s75, v232\n"
    "s_add_u32 s77, s97, 1\n"
    "s_and_b32 s77, s77, 7\n"
    "s_mul_i32 s87, s77, 0x30000\n"
    "s_add_u32 s87, s87, s82\n"
    "s_add_u32 m0, s83, 0x2000\n"
    "s_nop 0\n"
    "buffer_load_dwordx4 v15, s[28:31], s87 offen lds\n"
    "s_add_u32 s77, s97, 1\n"
    "s_and_b32 s77, s77, 7\n"
    "s_lshl_b32 s86, s77, 7\n"
    "s_add_u32 s86, s86, s80\n"
    "s_add_u32 m0, s81, 0x1000\n"
    "s_nop 0\n"
    "buffer_load_dwordx4 v16, s[24:27], s86 offen lds\n"
    "s_add_u32 s66, s86, 0x1c00\n"
    "buffer_load_dwordx4 v17, s[24:27], s66 offen offset:1024 lds\n"
    "s_add_u32 m0, s83, 0x2400\n"
    "s_add_u32 s65, s87, 0x3000\n"
    "buffer_load_dwordx4 v15, s[28:31], s65 offen lds\n"
    "s_add_u32 m0, s81, 0x1800\n"
    "s_addk_i32 s66, 9216\n"
    "buffer_load_dwordx4 v16, s[24:27], s66 offen lds\n"
    "s_addk_i32 s66, 7168\n"
    "buffer_load_dwordx4 v17, s[24:27], s66 offen offset:1024 lds\n"
    "s_add_u32 s77, s97, 2\n"
    "s_and_b32 s77, s77, 7\n"
    "s_mul_i32 s87, s77, 0x30000\n"
    "s_add_u32 s87, s87, s82\n"
    "s_add_u32 m0, s83, 0x4000\n"
    "s_nop 0\n"
    "buffer_load_dwordx4 v15, s[28:31], s87 offen lds\n"
    "s_add_u32 s77, s97, 2\n"
    "s_and_b32 s77, s77, 7\n"
    "s_lshl_b32 s86, s77, 7\n"
    "s_add_u32 s86, s86, s80\n"
    "s_add_u32 m0, s81, 0x2000\n"
    "s_nop 0\n"
    "buffer_load_dwordx4 v16, s[24:27], s86 offen lds\n"
    "s_add_u32 s66, s86, 0x1c00\n"
    "buffer_load_dwordx4 v17, s[24:27], s66 offen offset:1024 lds\n"
    "s_add_u32 m0, s83, 0x4400\n"
    "s_add_u32 s65, s87, 0x3000\n"
    "buffer_load_dwordx4 v15, s[28:31], s65 offen lds\n"
    "s_add_u32 m0, s81, 0x2800\n"
    "s_addk_i32 s66, 9216\n"
    "buffer_load_dwordx4 v16, s[24:27], s66 offen lds\n"
    "s_addk_i32 s66, 7168\n"
    "buffer_load_dwordx4 v17, s[24:27], s66 offen offset:1024 lds\n"
    "s_lshl_b32 s18, s57, 18\n"
    "s_waitcnt vmcnt(15)\n"
    "s_barrier\n"
    "ds_read_b128 a[0:3], v18\n"
    "ds_read_b128 a[64:67], v18 offset:2048\n"
    "ds_read_b64_tr_b16 v[80:81], v232\n"
    "ds_read_b64_tr_b16 v[82:83], v232 offset:1024\n"
    "ds_read_b64_tr_b16 v[64:65], v2\n"
    "ds_read_b64_tr_b16 v[66:67], v2 offset:1024\n"
    "ds_read_b64_tr_b16 v[68:69], v3\n"
    "ds_read_b64_tr_b16 v[70:71], v3 offset:1024\n"
    "ds_read_b64_tr_b16 v[72:73], v2 offset:2048\n"
    "ds_read_b64_tr_b16 v[74:75], v2 offset:3072\n"
    "ds_read_b64_tr_b16 v[76:77], v3 offset:2048\n"
    "ds_read_b64_tr_b16 v[78:79], v3 offset:3072\n"
    "s_waitcnt lgkmcnt(8)\n"
    "v_mfma_f32_16x16x32_bf16 v[144:147], v[24:27], v[80:83], 0\n"
    "s_waitcnt lgkmcnt(6)\n"
    "v_mfma_f32_16x16x32_bf16 v[112:115], a[0:3], v[64:67], 0\n"
    "ds_read_b128 a[4:7], v19\n"
    "ds_read_b128 a[68:71], v19 offset:2048\n"
    "v_mfma_f32_16x16x32_bf16 v[120:123], a[64:67], v[64:67], 0\n"
    "ds_read_b64_tr_b16 v[100:101], v232 offset:4096\n"
    "ds_read_b64_tr_b16 v[102:103], v232 offset:5120\n"
    "s_waitcnt lgkmcnt(8)\n"
    "v_mfma_f32_16x16x32_bf16 v[116:119], a[0:3], v[68:71], 0\n"
    "s_add_u32 s77, s97, 3\n"
    "s_and_b32 s77, s77, 7\n"
    "s_mul_i32 s87, s77, 0x30000\n"
    "s_add_u32 s87, s87, s82\n"
    "s_add_u32 m0, s83, 0x6000\n"
    "s_nop 0\n"
    "buffer_load_dwordx4 v15, s[28:31], s87 offen lds\n"
    "ds_read_b64_tr_b16 v[84:85], v2 offset:4096\n"
    "ds_read_b64_tr_b16 v[86:87], v2 offset:5120\n"
    "v_mfma_f32_16x16x32_bf16 v[124:127], a[64:67], v[68:71], 0\n"
    "ds_read_b64_tr_b16 v[88:89], v3 offset:4096\n"
    "s_waitcnt lgkmcnt(9)\n"
    "v_mfma_f32_16x16x32_bf16 v[128:131], a[0:3], v[72:75], 0\n"
    "s_add_u32 s77, s97, 3\n"
    "s_and_b32 s77, s77, 7\n"
    "s_lshl_b32 s86, s77, 7\n"
    "s_add_u32 s86, s86, s80\n"
    "s_add_u32 m0, s81, 0x3000\n"
    "s_nop 0\n"
    "buffer_load_dwordx4 v16, s[24:27], s86 offen lds\n"
    "ds_read_b64_tr_b16 v[90:91], v3 offset:5120\n"
    "ds_read_b64_tr_b16 v[92:93], v2 offset:6144\n"
    "v_mfma_f32_16x16x32_bf16 v[136:139], a[64:67], v[72:75], 0\n"
    "ds_read_b64_tr_b16 v[94:95], v2 offset:7168\n"
    "ds_read_b64_tr_b16 v[96:97], v3 offset:6144\n"
    "s_waitcnt lgkmcnt(11)\n"
    "v_mfma_f32_16x16x32_bf16 v[132:135], a[0:3], v[76:79], 0\n"
    "s_add_u32 s66, s86, 0x1c00\n"
    "buffer_load_dwordx4 v17, s[24:27], s66 offen offset:1024 lds\n"
    "ds_read_b64_tr_b16 v[98:99], v3 offset:7168\n"
    "v_mfma_f32_16x16x32_bf16 v[140:143], a[64:67], v[76:79], 0\n"
    "s_waitcnt lgkmcnt(8)\n"
    "v_mfma_f32_16x16x32_bf16 v[144:147], v[24:27], v[100:103], v[144:147]\n"
    "s_waitcnt vmcnt(17)\n"
    "v_readfirstlane_b32 s75, v60\n"
    "s_nop 0\n"
    "s_mul_i32 s62, s75, 0x9e3779b1\n"
    "s_add_u32 s62, s62, 0x7f4a7c15\n"
    "s_mov_b32 s8, 0x3b000000\n"
    "s_mov_b32 s10, 0x3fb8aa3b\n"
    "s_waitcnt vmcnt(16)\n"
    "v_lshlrev_b32 v204, 16, v196\n"
    "s_waitcnt lgkmcnt(6)\n"
    "v_mfma_f32_16x16x32_bf16 v[112:115], a[4:7], v[84:87], v[112:115]\n"
    "s_waitcnt vmcnt(9)\n"
    "s_barrier\n"
    "ds_read_b128 a[8:11], v18 offset:4096\n"
    "ds_read_b128 a[72:75], v18 offset:6144\n"
    "v_and_b32 v205, 0xffff0000, v196\n"
    "v_lshlrev_b32 v206, 16, v197\n"
    "v_mfma_f32_16x16x32_bf16 v[120:123], a[68:71], v[84:87], v[120:123]\n"
    "ds_read_b64_tr_b16 v[80:81], v232 offset:8192\n"
    "ds_read_b64_tr_b16 v[82:83], v232 offset:9216\n"
    "v_and_b32 v207, 0xffff0000, v197\n"
    "v_lshlrev_b32 v208, 16, v198\n"
    "s_waitcnt lgkmcnt(8)\n"
    "v_mfma_f32_16x16x32_bf16 v[116:119], a[4:7], v[88:91], v[116:119]\n"
    "s_add_u32 m0, s83, 0x6400\n"
    "s_add_u32 s65, s87, 0x3000\n"
    "buffer_load_dwordx4 v15, s[28:31], s65 offen lds\n"
    "ds_read_b64_tr_b16 v[64:65], v2 offset:8192\n"
    "ds_read_b64_tr_b16 v[66:67], v2 offset:9216\n"
    "v_and_b32 v209, 0xffff0000, v198\n"
    "v_lshlrev_b32 v210, 16, v199\n"
    "v_mfma_f32_16x16x32_bf16 v[124:127], a[68:71], v[88:91], v[124:127]\n"
    "ds_read_b64_tr_b16 v[68:69], v3 offset:8192\n"
    "v_and_b32 v211, 0xffff0000, v199\n"
    "v_pk_add_f32 v[220:221], v[204:205], v[206:207]\n"
    "s_waitcnt lgkmcnt(9)\n"
    "v_mfma_f32_16x16x32_bf16 v[128:131], a[4:7], v[92:95], v[128:131]\n"
    "s_add_u32 m0, s81, 0x3800\n"
    "s_addk_i32 s66, 9216\n"
    "buffer_load_dwordx4 v16, s[24:27], s66 offen lds\n"
    "ds_read_b64_tr_b16 v[70:71], v3 offset:9216\n"
    "ds_read_b64_tr_b16 v[72:73], v2 offset:10240\n"
    "v_pk_mul_f32 v[224:225], v[204:205], v[204:205]\n"
    "v_pk_add_f32 v[220:221], v[220:221], v[208:209]\n"
    "v_mfma_f32_16x16x32_bf16 v[136:139], a[68:71], v[92:95], v[136:139]\n"
    "ds_read_b64_tr_b16 v[74:75], v2 offset:11264\n"
    "ds_read_b64_tr_b16 v[76:77], v3 offset:10240\n"
    "v_pk_fma_f32 v[224:225], v[206:207], v[206:207], v[224:225]\n"
    "v_pk_add_f32 v[220:221], v[220:221], v[210:211]\n"
    "s_waitcnt lgkmcnt(11)\n"
    "v_mfma_f32_16x16x32_bf16 v[132:135], a[4:7], v[96:99], v[132:135]\n"
    "s_addk_i32 s66, 7168\n"
    "buffer_load_dwordx4 v17, s[24:27], s66 offen offset:1024 lds\n"
    "ds_read_b64_tr_b16 v[78:79], v3 offset:11264\n"
    "v_pk_fma_f32 v[224:225], v[208:209], v[208:209], v[224:225]\n"
    "v_pk_fma_f32 v[224:225], v[210:211], v[210:211], v[224:225]\n"
    "v_mfma_f32_16x16x32_bf16 v[140:143], a[68:71], v[96:99], v[140:143]\n"
    "v_add_f32 v220, v220, v221\n"
    "v_add_f32 v224, v224, v225\n"
    "s_waitcnt lgkmcnt(8)\n"
    "v_mfma_f32_16x16x32_bf16 v[144:147], v[24:27], v[80:83], v[144:147]\n"
    "v_lshlrev_b32 v212, 16, v200\n"
    "v_and_b32 v213, 0xffff0000, v200\n"
    "s_waitcnt lgkmcnt(6)\n"
    "v_mfma_f32_16x16x32_bf16 v[112:115], a[8:11], v[64:67], v[112:115]\n"
    "ds_read_b128 a[12:15], v19 offset:4096\n"
    "ds_read_b128 a[76:79], v19 offset:6144\n"
    "v_lshlrev_b32 v214, 16, v201\n"
    "v_and_b32 v215, 0xffff0000, v201\n"
    "v_mfma_f32_16x16x32_bf16 v[120:123], a[72:75], v[64:67], v[120:123]\n"
    "ds_read_b64_tr_b16 v[100:101], v232 offset:12288\n"
    "ds_read_b64_tr_b16 v[102:103], v232 offset:13312\n"
    "v_lshlrev_b32 v216, 16, v202\n"
    "v_and_b32 v217, 0xffff0000, v202\n"
    "s_waitcnt lgkmcnt(8)\n"
    "v_mfma_f32_16x16x32_bf16 v[116:119], a[8:11], v[68:71], v[116:119]\n"
    "s_add_u32 s77, s97, 4\n"
    "s_and_b32 s77, s77, 7\n"
    "s_mul_i32 s87, s77, 0x30000\n"
    "s_add_u32 s87, s87, s82\n"
    "s_add_u32 m0, s83, 0x8000\n"
    "s_nop 0\n"
    "buffer_load_dwordx4 v15, s[28:31], s87 offen lds\n"
    "ds_read_b64_tr_b16 v[84:85], v2 offset:12288\n"
    "ds_read_b64_tr_b16 v[86:87], v2 offset:13312\n"
    "v_lshlrev_b32 v218, 16, v203\n"
    "v_and_b32 v219, 0xffff0000, v203\n"
    "v_mfma_f32_16x16x32_bf16 v[124:127], a[72:75], v[68:71], v[124:127]\n"
    "ds_read_b64_tr_b16 v[88:89], v3 offset:12288\n"
    "v_pk_add_f32 v[222:223], v[212:213], v[214:215]\n"
    "v_pk_mul_f32 v[226:227], v[212:213], v[212:213]\n"
    "s_waitcnt lgkmcnt(9)\n"
    "v_mfma_f32_16x16x32_bf16 v[128:131], a[8:11], v[72:75], v[128:131]\n"
    "s_add_u32 s77, s97, 4\n"
    "s_and_b32 s77, s77, 7\n"
    "s_lshl_b32 s86, s77, 7\n"
    "s_add_u32 s86, s86, s80\n"
    "s_add_u32 m0, s81, 0x4000\n"
    "s_nop 0\n"
    "buffer_load_dwordx4 v16, s[24:27], s86 offen lds\n"
    "ds_read_b64_tr_b16 v[90:91], v3 offset:13312\n"
    "ds_read_b64_tr_b16 v[92:93], v2 offset:14336\n"
    "v_pk_add_f32 v[222:223], v[222:223], v[216:217]\n"
    "v_pk_fma_f32 v[226:227], v[214:215], v[214:215], v[226:227]\n"
    "v_mfma_f32_16x16x32_bf16 v[136:139], a[72:75], v[72:75], v[136:139]\n"
    "ds_read_b64_tr_b16 v[94:95], v2 offset:15360\n"
    "ds_read_b64_tr_b16 v[96:97], v3 offset:14336\n"
    "v_pk_add_f32 v[222:223], v[222:223], v[218:219]\n"
    "v_pk_fma_f32 v[226:227], v[216:217], v[216:217], v[226:227]\n"
    "s_waitcnt lgkmcnt(11)\n"
    "v_mfma_f32_16x16x32_bf16 v[132:135], a[8:11], v[76:79], v[132:135]\n"
    "s_add_u32 s66, s86, 0x1c00\n"
    "buffer_load_dwordx4 v17, s[24:27], s66 offen offset:1024 lds\n"
    "ds_read_b64_tr_b16 v[98:99], v3 offset:15360\n"
    "v_pk_fma_f32 v[226:227], v[218:219], v[218:219], v[226:227]\n"
    "v_add_f32 v222, v222, v223\n"
    "v_mfma_f32_16x16x32_bf16 v[140:143], a[72:75], v[76:79], v[140:143]\n"
    "v_add_f32 v226, v226, v227\n"
    "v_add_f32_dpp v220, v220, v220 row_ror:8 row_mask:0xf bank_mask:0xf\n"
    "s_waitcnt lgkmcnt(8)\n"
    "v_mfma_f32_16x16x32_bf16 v[144:147], v[24:27], v[100:103], v[144:147]\n"
    "v_add_f32_dpp v224, v224, v224 row_ror:8 row_mask:0xf bank_mask:0xf\n"
    "v_add_f32_dpp v222, v222, v222 row_ror:8 row_mask:0xf bank_mask:0xf\n"
    "s_waitcnt lgkmcnt(6)\n"
    "v_mfma_f32_16x16x32_bf16 v[112:115], a[12:15], v[84:87], v[112:115]\n"
    "s_waitcnt vmcnt(9)\n"
    "s_barrier\n"
    "ds_read_b128 a[16:19], v18 offset:8192\n"
    "ds_read_b128 a[80:83], v18 offset:10240\n"
    "v_add_f32_dpp v226, v226, v226 row_ror:8 row_mask:0xf bank_mask:0xf\n"
    "v_add_f32_dpp v220, v220, v220 row_ror:4 row_mask:0xf bank_mask:0xf\n"
    "v_mfma_f32_16x16x32_bf16 v[120:123], a[76:79], v[84:87], v[120:123]\n"
    "ds_read_b64_tr_b16 v[80:81], v232 offset:16384\n"
    "ds_read_b64_tr_b16 v[82:83], v232 offset:17408\n"
    "v_add_f32_dpp v224, v224, v224 row_ror:4 row_mask:0xf bank_mask:0xf\n"
    "v_add_f32_dpp v222, v222, v222 row_ror:4 row_mask:0xf bank_mask:0xf\n"
    "s_waitcnt lgkmcnt(8)\n"
    "v_mfma_f32_16x16x32_bf16 v[116:119], a[12:15], v[88:91], v[116:119]\n"
    "s_add_u32 m0, s83, 0x8400\n"
    "s_add_u32 s65, s87, 0x3000\n"
    "buffer_load_dwordx4 v15, s[28:31], s65 offen lds\n"
    "ds_read_b64_tr_b16 v[64:65], v2 offset:16384\n"
    "ds_read_b64_tr_b16 v[66:67], v2 offset:17408\n"
    "v_add_f32_dpp v226, v226, v226 row_ror:4 row_mask:0xf bank_mask:0xf\n"
    "v_add_f32_dpp v220, v220, v220 row_ror:2 row_mask:0xf bank_mask:0xf\n"
    "v_mfma_f32_16x16x32_bf16 v[124:127], a[76:79], v[88:91], v[124:127]\n"
    "ds_read_b64_tr_b16 v[68:69], v3 offset:16384\n"
    "v_add_f32_dpp v224, v224, v224 row_ror:2 row_mask:0xf bank_mask:0xf\n"
    "v_add_f32_dpp v222, v222, v222 row_ror:2 row_mask:0xf bank_mask:0xf\n"
    "s_waitcnt lgkmcnt(9)\n"
    "v_mfma_f32_16x16x32_bf16 v[128:131], a[12:15], v[92:95], v[128:131]\n"
    "s_add_u32 m0, s81, 0x4800\n"
    "s_addk_i32 s66, 9216\n"
    "buffer_load_dwordx4 v16, s[24:27], s66 offen lds\n"
    "ds_read_b64_tr_b16 v[70:71], v3 offset:17408\n"
    "ds_read_b64_tr_b16 v[72:73], v2 offset:18432\n"
    "v_add_f32_dpp v226, v226, v226 row_ror:2 row_mask:0xf bank_mask:0xf\n"
    "v_add_f32_dpp v220, v220, v220 row_ror:1 row_mask:0xf bank_mask:0xf\n"
    "v_mfma_f32_16x16x32_bf16 v[136:139], a[76:79], v[92:95], v[136:139]\n"
    "ds_read_b64_tr_b16 v[74:75], v2 offset:19456\n"
    "ds_read_b64_tr_b16 v[76:77], v3 offset:18432\n"
    "v_add_f32_dpp v224, v224, v224 row_ror:1 row_mask:0xf bank_mask:0xf\n"
    "v_add_f32_dpp v222, v222, v222 row_ror:1 row_mask:0xf bank_mask:0xf\n"
    "s_waitcnt lgkmcnt(11)\n"
    "v_mfma_f32_16x16x32_bf16 v[132:135], a[12:15], v[96:99], v[132:135]\n"
    "s_addk_i32 s66, 7168\n"
    "buffer_load_dwordx4 v17, s[24:27], s66 offen offset:1024 lds\n"
    "ds_read_b64_tr_b16 v[78:79], v3 offset:19456\n"
    "v_add_f32_dpp v226, v226, v226 row_ror:1 row_mask:0xf bank_mask:0xf\n"
    "v_permlane32_swap_b32 v220, v222\n"
    "v_mfma_f32_16x16x32_bf16 v[140:143], a[76:79], v[96:99], v[140:143]\n"
    "v_permlane32_swap_b32 v224, v226\n"
    "v_add_f32 v220, v220, v222\n"
    "s_waitcnt lgkmcnt(8)\n"
    "v_mfma_f32_16x16x32_bf16 v[144:147], v[24:27], v[80:83], v[144:147]\n"
    "v_add_f32 v224, v224, v226\n"
    "v_mov_b32 v222, v220\n"
    "s_waitcnt lgkmcnt(6)\n"
    "v_mfma_f32_16x16x32_bf16 v[112:115], a[16:19], v[64:67], v[112:115]\n"
    "ds_read_b128 a[20:23], v19 offset:8192\n"
    "ds_read_b128 a[84:87], v19 offset:10240\n"
    "v_mov_b32 v226, v224\n"
    "v_permlane16_swap_b32 v220, v222\n"
    "v_mfma_f32_16x16x32_bf16 v[120:123], a[80:83], v[64:67], v[120:123]\n"
    "ds_read_b64_tr_b16 v[100:101], v232 offset:20480\n"
    "ds_read_b64_tr_b16 v[102:103], v232 offset:21504\n"
    "v_permlane16_swap_b32 v224, v226\n"
    "v_add_f32 v220, v220, v222\n"
    "s_waitcnt lgkmcnt(8)\n"
    "v_mfma_f32_16x16x32_bf16 v[116:119], a[16:19], v[68:71], v[116:119]\n"
    "s_add_u32 s77, s97, 5\n"
    "s_and_b32 s77, s77, 7\n"
    "s_mul_i32 s87, s77, 0x30000\n"
    "s_add_u32 s87, s87, s82\n"
    "s_add_u32 m0, s83, 0xa000\n"
    "s_nop 0\n"
    "buffer_load_dwordx4 v15, s[28:31], s87 offen lds\n"
    "ds_read_b64_tr_b16 v[84:85], v2 offset:20480\n"
    "ds_read_b64_tr_b16 v[86:87], v2 offset:21504\n"
    "v_add_f32 v224, v224, v226\n"
    "v_mul_f32 v228, s8, v220\n"
    "v_mfma_f32_16x16x32_bf16 v[124:127], a[80:83], v[68:71], v[124:127]\n"
    "ds_read_b64_tr_b16 v[88:89], v3 offset:20480\n"
    "v_mul_f32 v224, s8, v224\n"
    "v_fma_f32 v224, -v228, v228, v224\n"
    "s_waitcnt lgkmcnt(9)\n"
    "v_mfma_f32_16x16x32_bf16 v[128:131], a[16:19], v[72:75], v[128:131]\n"
    "s_add_u32 s77, s97, 5\n"
    "s_and_b32 s77, s77, 7\n"
    "s_lshl_b32 s86, s77, 7\n"
    "s_add_u32 s86, s86, s80\n"
    "s_add_u32 m0, s81, 0x5000\n"
    "s_nop 0\n"
    "buffer_load_dwordx4 v16, s[24:27], s86 offen lds\n"
    "ds_read_b64_tr_b16 v[90:91], v3 offset:21504\n"
    "ds_read_b64_tr_b16 v[92:93], v2 offset:22528\n"
    "v_max_f32 v224, 0, v224\n"
    "v_add_f32 v224, 0x3727c5ac, v224\n"
    "v_mfma_f32_16x16x32_bf16 v[136:139], a[80:83], v[72:75], v[136:139]\n"
    "ds_read_b64_tr_b16 v[94:95], v2 offset:23552\n"
    "ds_read_b64_tr_b16 v[96:97], v3 offset:22528\n"
    "v_rsq_f32 v230, v224\n"
    "v_mov_b32 v229, s62\n"
    "s_waitcnt lgkmcnt(11)\n"
    "v_mfma_f32_16x16x32_bf16 v[132:135], a[16:19], v[76:79], v[132:135]\n"
    "s_add_u32 s66, s86, 0x1c00\n"
    "buffer_load_dwordx4 v17, s[24:27], s66 offen offset:1024 lds\n"
    "ds_read_b64_tr_b16 v[98:99], v3 offset:23552\n"
    "v_mov_b32 v231, s62\n"
    "v_lshrrev_b32 v61, 5, v23\n"
    "v_lshlrev_b32 v61, 6, v61\n"
    "v_lshl_add_u32 v61, v21, 4, v61\n"
    "s_lshl_b32 s75, s96, 6\n"
    "s_mov_b32 exec_lo, 0xf\n"
    "s_mov_b32 exec_hi, 0xf\n"
    "s_add_u32 s67, s75, 0x540000\n"
    "buffer_store_dwordx4 v[228:231], v61, s[52:55], s67 offen sc1\n"
    "s_mov_b64 exec, -1\n"
    "v_mfma_f32_16x16x32_bf16 v[140:143], a[80:83], v[76:79], v[140:143]\n"
    "s_not_b32 s75, s62\n"
    "v_mov_b32 v236, s75\n"
    "v_mov_b32 v237, s75\n"
    "v_mov_b32 v238, s75\n"
    "v_mov_b32 v239, s75\n"
    "s_lshl_b32 s75, s57, 14\n"
    "s_lshl2_add_u32 s76, s58, s59\n"
    "s_lshl_b32 s76, s76, 8\n"
    "s_add_u32 s75, s75, s76\n"
    "v_lshrrev_b32 v61, 3, v23\n"
    "v_lshlrev_b32 v61, 18, v61\n"
    "v_and_b32 v62, 7, v23\n"
    "v_lshl_add_u32 v61, v62, 4, v61\n"
    "s_mov_b32 exec_lo, 0xffffff\n"
    "s_mov_b32 exec_hi, 0x0\n"
    "s_add_u32 s67, s75, 0xd00000\n"
    "buffer_store_dwordx4 v[236:239], v61, s[52:55], s67 offen sc1\n"
    "s_mov_b64 exec, -1\n"
    "s_waitcnt lgkmcnt(8)\n"
    "v_mfma_f32_16x16x32_bf16 v[144:147], v[24:27], v[100:103], v[144:147]\n"
    "s_waitcnt lgkmcnt(6)\n"
    "v_mfma_f32_16x16x32_bf16 v[112:115], a[20:23], v[84:87], v[112:115]\n"
    "s_waitcnt vmcnt(11)\n"
    "s_barrier\n"
    "ds_read_b128 a[24:27], v18 offset:12288\n"
    "ds_read_b128 a[88:91], v18 offset:14336\n"
    "v_mfma_f32_16x16x32_bf16 v[120:123], a[84:87], v[84:87], v[120:123]\n"
    "ds_read_b64_tr_b16 v[80:81], v232 offset:24576\n"
    "ds_read_b64_tr_b16 v[82:83], v232 offset:25600\n"
    "s_waitcnt lgkmcnt(8)\n"
    "v_mfma_f32_16x16x32_bf16 v[116:119], a[20:23], v[88:91], v[116:119]\n"
    "s_add_u32 m0, s83, 0xa400\n"
    "s_add_u32 s65, s87, 0x3000\n"
    "buffer_load_dwordx4 v15, s[28:31], s65 offen lds\n"
    "ds_read_b64_tr_b16 v[64:65], v2 offset:24576\n"
    "ds_read_b64_tr_b16 v[66:67], v2 offset:25600\n"
    "v_mfma_f32_16x16x32_bf16 v[124:127], a[84:87], v[88:91], v[124:127]\n"
    "ds_read_b64_tr_b16 v[68:69], v3 offset:24576\n"
    "s_waitcnt lgkmcnt(9)\n"
    "v_mfma_f32_16x16x32_bf16 v[128:131], a[20:23], v[92:95], v[128:131]\n"
    "s_add_u32 m0, s81, 0x5800\n"
    "s_addk_i32 s66, 9216\n"
    "buffer_load_dwordx4 v16, s[24:27], s66 offen lds\n"
    "ds_read_b64_tr_b16 v[70:71], v3 offset:25600\n"
    "ds_read_b64_tr_b16 v[72:73], v2 offset:26624\n"
    "v_mfma_f32_16x16x32_bf16 v[136:139], a[84:87], v[92:95], v[136:139]\n"
    "ds_read_b64_tr_b16 v[74:75], v2 offset:27648\n"
    "ds_read_b64_tr_b16 v[76:77], v3 offset:26624\n"
    "s_waitcnt lgkmcnt(11)\n"
    "v_mfma_f32_16x16x32_bf16 v[132:135], a[20:23], v[96:99], v[132:135]\n"
    "s_addk_i32 s66, 7168\n"
    "buffer_load_dwordx4 v17, s[24:27], s66 offen offset:1024 lds\n"
    "ds_read_b64_tr_b16 v[78:79], v3 offset:27648\n"
    "v_mfma_f32_16x16x32_bf16 v[140:143], a[84:87], v[96:99], v[140:143]\n"
    "s_waitcnt lgkmcnt(8)\n"
    "v_mfma_f32_16x16x32_bf16 v[144:147], v[24:27], v[80:83], v[144:147]\n"
    "s_waitcnt lgkmcnt(6)\n"
    "v_mfma_f32_16x16x32_bf16 v[112:115], a[24:27], v[64:67], v[112:115]\n"
    "ds_read_b128 a[28:31], v19 offset:12288\n"
    "ds_read_b128 a[92:95], v19 offset:14336\n"
    "v_mfma_f32_16x16x32_bf16 v[120:123], a[88:91], v[64:67], v[120:123]\n"
    "ds_read_b64_tr_b16 v[100:101], v232 offset:28672\n"
    "ds_read_b64_tr_b16 v[102:103], v232 offset:29696\n"
    "s_waitcnt lgkmcnt(8)\n"
    "v_mfma_f32_16x16x32_bf16 v[116:119], a[24:27], v[68:71], v[116:119]\n"
    "s_add_u32 s77, s97, 6\n"
    "s_and_b32 s77, s77, 7\n"
    "s_mul_i32 s87, s77, 0x30000\n"
    "s_add_u32 s87, s87, s82\n"
    "s_add_u32 m0, s83, 0xc000\n"
    "s_nop 0\n"
    "buffer_load_dwordx4 v15, s[28:31], s87 offen lds\n"
    "ds_read_b64_tr_b16 v[84:85], v2 offset:28672\n"
    "ds_read_b64_tr_b16 v[86:87], v2 offset:29696\n"
    "v_mfma_f32_16x16x32_bf16 v[124:127], a[88:91], v[68:71], v[124:127]\n"
    "ds_read_b64_tr_b16 v[88:89], v3 offset:28672\n"
    "s_waitcnt lgkmcnt(9)\n"
    "v_mfma_f32_16x16x32_bf16 v[128:131], a[24:27], v[72:75], v[128:131]\n"
    "s_add_u32 s77, s97, 6\n"
    "s_and_b32 s77, s77, 7\n"
    "s_lshl_b32 s86, s77, 7\n"
    "s_add_u32 s86, s86, s80\n"
    "s_add_u32 m0, s81, 0\n"
    "s_nop 0\n"
    "buffer_load_dwordx4 v16, s[24:27], s86 offen lds\n"
    "ds_read_b64_tr_b16 v[90:91], v3 offset:29696\n"
    "ds_read_b64_tr_b16 v[92:93], v2 offset:30720\n"
    "v_mfma_f32_16x16x32_bf16 v[136:139], a[88:91], v[72:75], v[136:139]\n"
    "ds_read_b64_tr_b16 v[94:95], v2 offset:31744\n"
    "ds_read_b64_tr_b16 v[96:97], v3 offset:30720\n"
    "s_waitcnt lgkmcnt(11)\n"
    "v_mfma_f32_16x16x32_bf16 v[132:135], a[24:27], v[76:79], v[132:135]\n"
    "s_add_u32 s66, s86, 0x1c00\n"
    "buffer_load_dwordx4 v17, s[24:27], s66 offen offset:1024 lds\n"
    "ds_read_b64_tr_b16 v[98:99], v3 offset:31744\n"
    "v_mfma_f32_16x16x32_bf16 v[140:143], a[88:91], v[76:79], v[140:143]\n"
    "s_waitcnt lgkmcnt(8)\n"
    "v_mfma_f32_16x16x32_bf16 v[144:147], v[24:27], v[100:103], v[144:147]\n"
    "s_waitcnt lgkmcnt(6)\n"
    "v_mfma_f32_16x16x32_bf16 v[112:115], a[28:31], v[84:87], v[112:115]\n"
    "s_waitcnt vmcnt(11)\n"
    "s_barrier\n"
    "ds_read_b128 a[32:35], v18 offset:16384\n"
    "ds_read_b128 a[96:99], v18 offset:18432\n"
    "v_mfma_f32_16x16x32_bf16 v[120:123], a[92:95], v[84:87], v[120:123]\n"
    "ds_read_b64_tr_b16 v[80:81], v232 offset:32768\n"
    "ds_read_b64_tr_b16 v[82:83], v232 offset:33792\n"
    "s_waitcnt lgkmcnt(8)\n"
    "v_mfma_f32_16x16x32_bf16 v[116:119], a[28:31], v[88:91], v[116:119]\n"
    "s_add_u32 m0, s83, 0xc400\n"
    "s_add_u32 s65, s87, 0x3000\n"
    "buffer_load_dwordx4 v15, s[28:31], s65 offen lds\n"
    "ds_read_b64_tr_b16 v[64:65], v2 offset:32768\n"
    "ds_read_b64_tr_b16 v[66:67], v2 offset:33792\n"
    "v_mfma_f32_16x16x32_bf16 v[124:127], a[92:95], v[88:91], v[124:127]\n"
    "ds_read_b64_tr_b16 v[68:69], v3 offset:32768\n"
    "s_waitcnt lgkmcnt(9)\n"
    "v_mfma_f32_16x16x32_bf16 v[128:131], a[28:31], v[92:95], v[128:131]\n"
    "s_add_u32 m0, s81, 0x800\n"
    "s_addk_i32 s66, 9216\n"
    "buffer_load_dwordx4 v16, s[24:27], s66 offen lds\n"
    "ds_read_b64_tr_b16 v[70:71], v3 offset:33792\n"
    "ds_read_b64_tr_b16 v[72:73], v2 offset:34816\n"
    "v_mfma_f32_16x16x32_bf16 v[136:139], a[92:95], v[92:95], v[136:139]\n"
    "ds_read_b64_tr_b16 v[74:75], v2 offset:35840\n"
    "ds_read_b64_tr_b16 v[76:77], v3 offset:34816\n"
    "s_waitcnt lgkmcnt(11)\n"
    "v_mfma_f32_16x16x32_bf16 v[132:135], a[28:31], v[96:99], v[132:135]\n"
    "s_addk_i32 s66, 7168\n"
    "buffer_load_dwordx4 v17, s[24:27], s66 offen offset:1024 lds\n"
    "ds_read_b64_tr_b16 v[78:79], v3 offset:35840\n"
    "v_mfma_f32_16x16x32_bf16 v[140:143], a[92:95], v[96:99], v[140:143]\n"
    "s_waitcnt lgkmcnt(8)\n"
    "v_mfma_f32_16x16x32_bf16 v[144:147], v[24:27], v[80:83], v[144:147]\n"
    "s_waitcnt lgkmcnt(6)\n"
    "v_mfma_f32_16x16x32_bf16 v[112:115], a[32:35], v[64:67], v[112:115]\n"
    "ds_read_b128 a[36:39], v19 offset:16384\n"
    "ds_read_b128 a[100:103], v19 offset:18432\n"
    "v_mfma_f32_16x16x32_bf16 v[120:123], a[96:99], v[64:67], v[120:123]\n"
    "ds_read_b64_tr_b16 v[100:101], v232 offset:36864\n"
    "ds_read_b64_tr_b16 v[102:103], v232 offset:37888\n"
    "s_waitcnt lgkmcnt(8)\n"
    "v_mfma_f32_16x16x32_bf16 v[116:119], a[32:35], v[68:71], v[116:119]\n"
    "s_add_u32 s77, s97, 7\n"
    "s_and_b32 s77, s77, 7\n"
    "s_mul_i32 s87, s77, 0x30000\n"
    "s_add_u32 s87, s87, s82\n"
    "s_add_u32 m0, s83, 0xe000\n"
    "s_nop 0\n"
    "buffer_load_dwordx4 v15, s[28:31], s87 offen lds\n"
    "ds_read_b64_tr_b16 v[84:85], v2 offset:36864\n"
    "ds_read_b64_tr_b16 v[86:87], v2 offset:37888\n"
    "v_mfma_f32_16x16x32_bf16 v[124:127], a[96:99], v[68:71], v[124:127]\n"
    "ds_read_b64_tr_b16 v[88:89], v3 offset:36864\n"
    "s_waitcnt lgkmcnt(9)\n"
    "v_mfma_f32_16x16x32_bf16 v[128:131], a[32:35], v[72:75], v[128:131]\n"
    "s_add_u32 s77, s97, 7\n"
    "s_and_b32 s77, s77, 7\n"
    "s_lshl_b32 s86, s77, 7\n"
    "s_add_u32 s86, s86, s80\n"
    "s_add_u32 m0, s81, 0x1000\n"
    "s_nop 0\n"
    "buffer_load_dwordx4 v16, s[24:27], s86 offen lds\n"
    "ds_read_b64_tr_b16 v[90:91], v3 offset:37888\n"
    "ds_read_b64_tr_b16 v[92:93], v2 offset:38912\n"
    "v_mfma_f32_16x16x32_bf16 v[136:139], a[96:99], v[72:75], v[136:139]\n"
    "ds_read_b64_tr_b16 v[94:95], v2 offset:39936\n"
    "ds_read_b64_tr_b16 v[96:97], v3 offset:38912\n"
    "s_waitcnt lgkmcnt(11)\n"
    "v_mfma_f32_16x16x32_bf16 v[132:135], a[32:35], v[76:79], v[132:135]\n"
    "s_add_u32 s66, s86, 0x1c00\n"
    "buffer_load_dwordx4 v17, s[24:27], s66 offen offset:1024 lds\n"
    "ds_read_b64_tr_b16 v[98:99], v3 offset:39936\n"
    "v_mfma_f32_16x16x32_bf16 v[140:143], a[96:99], v[76:79], v[140:143]\n"
    "s_waitcnt lgkmcnt(8)\n"
    "v_mfma_f32_16x16x32_bf16 v[144:147], v[24:27], v[100:103], v[144:147]\n"
    "s_waitcnt lgkmcnt(6)\n"
    "v_mfma_f32_16x16x32_bf16 v[112:115], a[36:39], v[84:87], v[112:115]\n"
    "s_waitcnt vmcnt(9)\n"
    "s_barrier\n"
    "ds_read_b128 a[40:43], v18 offset:20480\n"
    "ds_read_b128 a[104:107], v18 offset:22528\n"
    "v_mfma_f32_16x16x32_bf16 v[120:123], a[100:103], v[84:87], v[120:123]\n"
    "ds_read_b64_tr_b16 v[80:81], v232 offset:40960\n"
    "ds_read_b64_tr_b16 v[82:83], v232 offset:41984\n"
    "s_waitcnt lgkmcnt(8)\n"
    "v_mfma_f32_16x16x32_bf16 v[116:119], a[36:39], v[88:91], v[116:119]\n"
    "s_add_u32 m0, s83, 0xe400\n"
    "s_add_u32 s65, s87, 0x3000\n"
    "buffer_load_dwordx4 v15, s[28:31], s65 offen lds\n"
    "ds_read_b64_tr_b16 v[64:65], v2 offset:40960\n"
    "ds_read_b64_tr_b16 v[66:67], v2 offset:41984\n"
    "v_mfma_f32_16x16x32_bf16 v[124:127], a[100:103], v[88:91], v[124:127]\n"
    "ds_read_b64_tr_b16 v[68:69], v3 offset:40960\n"
    "s_waitcnt lgkmcnt(9)\n"
    "v_mfma_f32_16x16x32_bf16 v[128:131], a[36:39], v[92:95], v[128:131]\n"
    "s_add_u32 m0, s81, 0x1800\n"
    "s_addk_i32 s66, 9216\n"
    "buffer_load_dwordx4 v16, s[24:27], s66 offen lds\n"
    "ds_read_b64_tr_b16 v[70:71], v3 offset:41984\n"
    "ds_read_b64_tr_b16 v[72:73], v2 offset:43008\n"
    "v_mfma_f32_16x16x32_bf16 v[136:139], a[100:103], v[92:95], v[136:139]\n"
    "ds_read_b64_tr_b16 v[74:75], v2 offset:44032\n"
    "ds_read_b64_tr_b16 v[76:77], v3 offset:43008\n"
    "s_waitcnt lgkmcnt(11)\n"
    "v_mfma_f32_16x16x32_bf16 v[132:135], a[36:39], v[96:99], v[132:135]\n"
    "s_addk_i32 s66, 7168\n"
    "buffer_load_dwordx4 v17, s[24:27], s66 offen offset:1024 lds\n"
    "ds_read_b64_tr_b16 v[78:79], v3 offset:44032\n"
    "v_mfma_f32_16x16x32_bf16 v[140:143], a[100:103], v[96:99], v[140:143]\n"
    "s_waitcnt lgkmcnt(8)\n"
    "v_mfma_f32_16x16x32_bf16 v[144:147], v[24:27], v[80:83], v[144:147]\n"
    "s_waitcnt lgkmcnt(6)\n"
    "v_mfma_f32_16x16x32_bf16 v[112:115], a[40:43], v[64:67], v[112:115]\n"
    "ds_read_b128 a[44:47], v19 offset:20480\n"
    "ds_read_b128 a[108:111], v19 offset:22528\n"
    "v_mfma_f32_16x16x32_bf16 v[120:123], a[104:107], v[64:67], v[120:123]\n"
    "ds_read_b64_tr_b16 v[100:101], v232 offset:45056\n"
    "ds_read_b64_tr_b16 v[102:103], v232 offset:46080\n"
    "s_waitcnt lgkmcnt(8)\n"
    "v_mfma_f32_16x16x32_bf16 v[116:119], a[40:43], v[68:71], v[116:119]\n"
    "s_lshr_b32 s88, s80, 4\n"
    "v_lshlrev_b32 v63, 6, v21\n"
    "s_add_u32 s67, s88, 0x540000\n"
    "buffer_load_dwordx4 v[156:159], v63, s[52:55], s67 offen sc1\n"
    "buffer_load_dwordx4 v[160:163], v63, s[52:55], s67 offen offset:1024 sc1\n"
    "ds_read_b64_tr_b16 v[84:85], v2 offset:45056\n"
    "ds_read_b64_tr_b16 v[86:87], v2 offset:46080\n"
    "v_mfma_f32_16x16x32_bf16 v[124:127], a[104:107], v[68:71], v[124:127]\n"
    "ds_read_b64_tr_b16 v[88:89], v3 offset:45056\n"
    "s_waitcnt lgkmcnt(9)\n"
    "v_mfma_f32_16x16x32_bf16 v[128:131], a[40:43], v[72:75], v[128:131]\n"
    "s_add_u32 s77, s97, 0\n"
    "s_and_b32 s77, s77, 7\n"
    "s_mul_i32 s87, s77, 0x30000\n"
    "s_add_u32 s87, s87, s84\n"
    "s_addk_i32 m0, 2048\n"
    "s_nop 0\n"
    "buffer_load_dwordx4 v15, s[28:31], s87 offen lds\n"
    "ds_read_b64_tr_b16 v[90:91], v3 offset:46080\n"
    "ds_read_b64_tr_b16 v[92:93], v2 offset:47104\n"
    "v_mfma_f32_16x16x32_bf16 v[136:139], a[104:107], v[72:75], v[136:139]\n"
    "ds_read_b64_tr_b16 v[94:95], v2 offset:48128\n"
    "ds_read_b64_tr_b16 v[96:97], v3 offset:47104\n"
    "s_waitcnt lgkmcnt(11)\n"
    "v_mfma_f32_16x16x32_bf16 v[132:135], a[40:43], v[76:79], v[132:135]\n"
    "ds_read_b64_tr_b16 v[98:99], v3 offset:48128\n"
    "v_mfma_f32_16x16x32_bf16 v[140:143], a[104:107], v[76:79], v[140:143]\n"
    "s_waitcnt lgkmcnt(8)\n"
    "v_mfma_f32_16x16x32_bf16 v[144:147], v[24:27], v[100:103], v[144:147]\n"
    "s_waitcnt lgkmcnt(6)\n"
    "v_mfma_f32_16x16x32_bf16 v[112:115], a[44:47], v[84:87], v[112:115]\n"
    "s_waitcnt vmcnt(9)\n"
    "s_barrier\n"
    "ds_read_b128 a[48:51], v18\n"
    "ds_read_b128 a[112:115], v18 offset:2048\n"
    "v_mfma_f32_16x16x32_bf16 v[120:123], a[108:111], v[84:87], v[120:123]\n"
    "ds_read_b64_tr_b16 v[80:81], v232 offset:49152\n"
    "ds_read_b64_tr_b16 v[82:83], v232 offset:50176\n"
    "s_waitcnt lgkmcnt(8)\n"
    "v_mfma_f32_16x16x32_bf16 v[116:119], a[44:47], v[88:91], v[116:119]\n"
    "s_add_u32 s77, s97, 1\n"
    "s_and_b32 s77, s77, 7\n"
    "s_mul_i32 s87, s77, 0x30000\n"
    "s_add_u32 s87, s87, s84\n"
    "s_addk_i32 m0, 1024\n"
    "s_nop 0\n"
    "buffer_load_dwordx4 v15, s[28:31], s87 offen lds\n"
    "ds_read_b64_tr_b16 v[64:65], v2 offset:49152\n"
    "ds_read_b64_tr_b16 v[66:67], v2 offset:50176\n"
    "v_mfma_f32_16x16x32_bf16 v[124:127], a[108:111], v[88:91], v[124:127]\n"
    "ds_read_b64_tr_b16 v[68:69], v3 offset:49152\n"
    "s_waitcnt lgkmcnt(9)\n"
    "v_mfma_f32_16x16x32_bf16 v[128:131], a[44:47], v[92:95], v[128:131]\n"
    "s_add_u32 s77, s97, 2\n"
    "s_and_b32 s77, s77, 7\n"
    "s_mul_i32 s87, s77, 0x30000\n"
    "s_add_u32 s87, s87, s84\n"
    "s_addk_i32 m0, 1024\n"
    "s_nop 0\n"
    "buffer_load_dwordx4 v15, s[28:31], s87 offen lds\n"
    "ds_read_b64_tr_b16 v[70:71], v3 offset:50176\n"
    "ds_read_b64_tr_b16 v[72:73], v2 offset:51200\n"
    "v_mfma_f32_16x16x32_bf16 v[136:139], a[108:111], v[92:95], v[136:139]\n"
    "ds_read_b64_tr_b16 v[74:75], v2 offset:52224\n"
    "ds_read_b64_tr_b16 v[76:77], v3 offset:51200\n"
    "s_waitcnt lgkmcnt(11)\n"
    "v_mfma_f32_16x16x32_bf16 v[132:135], a[44:47], v[96:99], v[132:135]\n"
    "ds_read_b64_tr_b16 v[78:79], v3 offset:52224\n"
    "v_mfma_f32_16x16x32_bf16 v[140:143], a[108:111], v[96:99], v[140:143]\n"
    "s_waitcnt lgkmcnt(8)\n"
    "v_mfma_f32_16x16x32_bf16 v[144:147], v[24:27], v[80:83], v[144:147]\n"
    "s_waitcnt lgkmcnt(6)\n"
    "v_mfma_f32_16x16x32_bf16 v[112:115], a[48:51], v[64:67], v[112:115]\n"
    "ds_read_b128 a[52:55], v19\n"
    "ds_read_b128 a[116:119], v19 offset:2048\n"
    "v_mfma_f32_16x16x32_bf16 v[120:123], a[112:115], v[64:67], v[120:123]\n"
    "ds_read_b64_tr_b16 v[100:101], v232 offset:53248\n"
    "ds_read_b64_tr_b16 v[102:103], v232 offset:54272\n"
    "s_waitcnt lgkmcnt(8)\n"
    "v_mfma_f32_16x16x32_bf16 v[116:119], a[48:51], v[68:71], v[116:119]\n"
    "s_add_u32 s77, s97, 3\n"
    "s_and_b32 s77, s77, 7\n"
    "s_mul_i32 s87, s77, 0x30000\n"
    "s_add_u32 s87, s87, s84\n"
    "s_addk_i32 m0, 1024\n"
    "s_nop 0\n"
    "buffer_load_dwordx4 v15, s[28:31], s87 offen lds\n"
    "ds_read_b64_tr_b16 v[84:85], v2 offset:53248\n"
    "ds_read_b64_tr_b16 v[86:87], v2 offset:54272\n"
    "v_mfma_f32_16x16x32_bf16 v[124:127], a[112:115], v[68:71], v[124:127]\n"
    "ds_read_b64_tr_b16 v[88:89], v3 offset:53248\n"
    "s_waitcnt lgkmcnt(9)\n"
    "v_mfma_f32_16x16x32_bf16 v[128:131], a[48:51], v[72:75], v[128:131]\n"
    "s_add_u32 s77, s97, 4\n"
    "s_and_b32 s77, s77, 7\n"
    "s_mul_i32 s87, s77, 0x30000\n"
    "s_add_u32 s87, s87, s84\n"
    "s_addk_i32 m0, 1024\n"
    "s_nop 0\n"
    "buffer_load_dwordx4 v15, s[28:31], s87 offen lds\n"
    "ds_read_b64_tr_b16 v[90:91], v3 offset:54272\n"
    "ds_read_b64_tr_b16 v[92:93], v2 offset:55296\n"
    "v_mfma_f32_16x16x32_bf16 v[136:139], a[112:115], v[72:75], v[136:139]\n"
    "ds_read_b64_tr_b16 v[94:95], v2 offset:56320\n"
    "ds_read_b64_tr_b16 v[96:97], v3 offset:55296\n"
    "s_waitcnt lgkmcnt(11)\n"
    "v_mfma_f32_16x16x32_bf16 v[132:135], a[48:51], v[76:79], v[132:135]\n"
    "ds_read_b64_tr_b16 v[98:99], v3 offset:56320\n"
    "v_mfma_f32_16x16x32_bf16 v[140:143], a[112:115], v[76:79], v[140:143]\n"
    "s_waitcnt lgkmcnt(8)\n"
    "v_mfma_f32_16x16x32_bf16 v[144:147], v[24:27], v[100:103], v[144:147]\n"
    "s_waitcnt lgkmcnt(6)\n"
    "v_mfma_f32_16x16x32_bf16 v[112:115], a[52:55], v[84:87], v[112:115]\n"
    "s_waitcnt vmcnt(7)\n"
    "s_barrier\n"
    "ds_read_b128 a[56:59], v18 offset:4096\n"
    "ds_read_b128 a[120:123], v18 offset:6144\n"
    "v_mfma_f32_16x16x32_bf16 v[120:123], a[116:119], v[84:87], v[120:123]\n"
    "ds_read_b64_tr_b16 v[80:81], v232 offset:57344\n"
    "ds_read_b64_tr_b16 v[82:83], v232 offset:58368\n"
    "s_waitcnt lgkmcnt(8)\n"
    "v_mfma_f32_16x16x32_bf16 v[116:119], a[52:55], v[88:91], v[116:119]\n"
    "s_add_u32 s77, s97, 5\n"
    "s_and_b32 s77, s77, 7\n"
    "s_mul_i32 s87, s77, 0x30000\n"
    "s_add_u32 s87, s87, s84\n"
    "s_addk_i32 m0, 1024\n"
    "s_nop 0\n"
    "buffer_load_dwordx4 v15, s[28:31], s87 offen lds\n"
    "ds_read_b64_tr_b16 v[64:65], v2 offset:57344\n"
    "ds_read_b64_tr_b16 v[66:67], v2 offset:58368\n"
    "v_mfma_f32_16x16x32_bf16 v[124:127], a[116:119], v[88:91], v[124:127]\n"
    "ds_read_b64_tr_b16 v[68:69], v3 offset:57344\n"
    "s_waitcnt lgkmcnt(9)\n"
    "v_mfma_f32_16x16x32_bf16 v[128:131], a[52:55], v[92:95], v[128:131]\n"
    "s_add_u32 s77, s97, 6\n"
    "s_and_b32 s77, s77, 7\n"
    "s_mul_i32 s87, s77, 0x30000\n"
    "s_add_u32 s87, s87, s84\n"
    "s_addk_i32 m0, 1024\n"
    "s_nop 0\n"
    "buffer_load_dwordx4 v15, s[28:31], s87 offen lds\n"
    "ds_read_b64_tr_b16 v[70:71], v3 offset:58368\n"
    "ds_read_b64_tr_b16 v[72:73], v2 offset:59392\n"
    "v_mfma_f32_16x16x32_bf16 v[136:139], a[116:119], v[92:95], v[136:139]\n"
    "ds_read_b64_tr_b16 v[74:75], v2 offset:60416\n"
    "ds_read_b64_tr_b16 v[76:77], v3 offset:59392\n"
    "s_waitcnt lgkmcnt(11)\n"
    "v_mfma_f32_16x16x32_bf16 v[132:135], a[52:55], v[96:99], v[132:135]\n"
    "ds_read_b64_tr_b16 v[78:79], v3 offset:60416\n"
    "v_mfma_f32_16x16x32_bf16 v[140:143], a[116:119], v[96:99], v[140:143]\n"
    "s_waitcnt lgkmcnt(8)\n"
    "v_mfma_f32_16x16x32_bf16 v[144:147], v[24:27], v[80:83], v[144:147]\n"
    "s_waitcnt lgkmcnt(6)\n"
    "v_mfma_f32_16x16x32_bf16 v[112:115], a[56:59], v[64:67], v[112:115]\n"
    "ds_read_b128 a[60:63], v19 offset:4096\n"
    "ds_read_b128 a[124:127], v19 offset:6144\n"
    "v_mfma_f32_16x16x32_bf16 v[120:123], a[120:123], v[64:67], v[120:123]\n"
    "ds_read_b64_tr_b16 v[100:101], v232 offset:61440\n"
    "ds_read_b64_tr_b16 v[102:103], v232 offset:62464\n"
    "s_waitcnt lgkmcnt(8)\n"
    "v_mfma_f32_16x16x32_bf16 v[116:119], a[56:59], v[68:71], v[116:119]\n"
    "s_add_u32 s77, s97, 7\n"
    "s_and_b32 s77, s77, 7\n"
    "s_mul_i32 s87, s77, 0x30000\n"
    "s_add_u32 s87, s87, s84\n"
    "s_addk_i32 m0, 1024\n"
    "s_nop 0\n"
    "buffer_load_dwordx4 v15, s[28:31], s87 offen lds\n"
    "ds_read_b64_tr_b16 v[84:85], v2 offset:61440\n"
    "ds_read_b64_tr_b16 v[86:87], v2 offset:62464\n"
    "v_mfma_f32_16x16x32_bf16 v[124:127], a[120:123], v[68:71], v[124:127]\n"
    "ds_read_b64_tr_b16 v[88:89], v3 offset:61440\n"
    "s_waitcnt lgkmcnt(9)\n"
    "v_mfma_f32_16x16x32_bf16 v[128:131], a[56:59], v[72:75], v[128:131]\n"
    "ds_read_b64_tr_b16 v[90:91], v3 offset:62464\n"
    "ds_read_b64_tr_b16 v[92:93], v2 offset:63488\n"
    "v_mfma_f32_16x16x32_bf16 v[136:139], a[120:123], v[72:75], v[136:139]\n"
    "ds_read_b64_tr_b16 v[94:95], v2 offset:64512\n"
    "ds_read_b64_tr_b16 v[96:97], v3 offset:63488\n"
    "s_waitcnt lgkmcnt(11)\n"
    "v_mfma_f32_16x16x32_bf16 v[132:135], a[56:59], v[76:79], v[132:135]\n"
    "ds_read_b64_tr_b16 v[98:99], v3 offset:64512\n"
    "v_mfma_f32_16x16x32_bf16 v[140:143], a[120:123], v[76:79], v[140:143]\n"
    "s_mov_b32 s69, 0\n"
    "s_waitcnt vmcnt(8)\n"
    "v_bitop3_b32 v61, v157, v159, s62 bitop3:0x7e\n"
    "v_bitop3_b32 v62, v161, v163, s62 bitop3:0x7e\n"
    "v_or_b32 v61, v61, v62\n"
    "v_cmp_ne_u32 vcc, 0, v61\n"
    "s_nop 0\n"
    "s_cmp_eq_u64 vcc, 0\n"
    "s_cbranch_scc1 .Ld3_%=\n"
    ".Lp1_%=:\n"
    "s_add_u32 s67, s88, 0x540000\n"
    "buffer_load_dwordx4 v[156:159], v63, s[52:55], s67 offen sc1\n"
    "buffer_load_dwordx4 v[160:163], v63, s[52:55], s67 offen offset:1024 sc1\n"
    "s_waitcnt vmcnt(0)\n"
    "v_bitop3_b32 v61, v157, v159, s62 bitop3:0x7e\n"
    "v_bitop3_b32 v62, v161, v163, s62 bitop3:0x7e\n"
    "v_or_b32 v61, v61, v62\n"
    "v_cmp_ne_u32 vcc, 0, v61\n"
    "s_nop 0\n"
    "s_cmp_eq_u64 vcc, 0\n"
    "s_cbranch_scc1 .Ld3_%=\n"
    "s_add_u32 s69, s69, 1\n"
    "s_cmp_lt_u32 s69, 0x20000\n"
    "s_cbranch_scc1 .Lp1_%=\n"
    "s_nop 7\n"
    ".Ld3_%=:\n"
    "s_lshl_b32 s75, s59, 9\n"
    "s_add_u32 s75, s75, 0x2400\n"
    "v_lshl_add_u32 v61, v21, 2, s75\n"
    "v_lshl_add_u32 v62, v22, 4, s75\n"
    "v_mul_f32 v164, s10, v158\n"
    "ds_write_b32 v61, v156\n"
    "ds_write_b32 v61, v158 offset:64\n"
    "ds_write_b32 v61, v164 offset:128\n"
    "v_mul_f32 v166, s10, v162\n"
    "ds_write_b32 v61, v160 offset:192\n"
    "ds_write_b32 v61, v162 offset:256\n"
    "ds_write_b32 v61, v166 offset:320\n"
    "ds_read_b128 v[168:171], v62\n"
    "ds_read_b128 v[176:179], v62 offset:64\n"
    "ds_read_b128 v[184:187], v62 offset:128\n"
    "ds_read_b128 v[172:175], v62 offset:192\n"
    "ds_read_b128 v[180:183], v62 offset:256\n"
    "ds_read_b128 v[188:191], v62 offset:320\n"
    "v_mov_b32 v192, 0x358637bd\n"
    "v_mov_b32 v193, 0x358637bd\n"
    "s_waitcnt lgkmcnt(15)\n"
    "v_mfma_f32_16x16x32_bf16 v[144:147], v[24:27], v[100:103], v[144:147]\n"
    "v_mfma_f32_16x16x32_bf16 v[112:115], a[60:63], v[84:87], v[112:115]\n"
    "v_mfma_f32_16x16x32_bf16 v[120:123], a[124:127], v[84:87], v[120:123]\n"
    "s_cmp_eq_u32 s59, 0\n"
    "s_cselect_b32 s92, s36, s44\n"
    "s_cselect_b32 s93, s37, s45\n"
    "s_cselect_b32 s94, s38, s46\n"
    "s_cselect_b32 s95, s39, s47\n"
    "s_cmp_eq_u32 s59, 2\n"
    "s_cselect_b32 s92, s32, s92\n"
    "s_cselect_b32 s93, s33, s93\n"
    "s_cselect_b32 s94, s34, s94\n"
    "s_cmp_lt_u32 s59, 2\n"
    "s_cselect_b32 s75, 43, 16\n"
    "s_mul_i32 s76, s75, s56\n"
    "s_cmp_eq_u32 s59, 3\n"
    "s_cselect_b32 s75, 0, s75\n"
    "s_lshl_b32 s76, s76, 7\n"
    "v_lshl_add_u32 v61, v23, 7, s76\n"
    "v_cmp_gt_u32 vcc, s75, v23\n"
    "v_mov_b32 v62, 0x7fff0000\n"
    "s_nop 0\n"
    "v_cndmask_b32 v61, v62, v61, vcc\n"
    "buffer_load_dword v252, v61, s[92:95], 0 offen\n"
    "v_mfma_f32_16x16x32_bf16 v[116:119], a[60:63], v[88:91], v[116:119]\n"
    "v_mfma_f32_16x16x32_bf16 v[124:127], a[124:127], v[88:91], v[124:127]\n"
    "s_waitcnt lgkmcnt(14)\n"
    "v_mfma_f32_16x16x32_bf16 v[128:131], a[60:63], v[92:95], v[128:131]\n"
    "v_mfma_f32_16x16x32_bf16 v[136:139], a[124:127], v[92:95], v[136:139]\n"
    "s_lshl_b32 s75, s59, 6\n"
    "v_lshl_add_u32 v61, v21, 2, s75\n"
    "ds_write_b32 v61, v144 offset:13568\n"
    "s_waitcnt lgkmcnt(13)\n"
    "v_mfma_f32_16x16x32_bf16 v[132:135], a[60:63], v[96:99], v[132:135]\n"
    "v_mfma_f32_16x16x32_bf16 v[140:143], a[124:127], v[96:99], v[140:143]\n"
    "s_waitcnt lgkmcnt(0)\n"
    "s_barrier\n"
    "v_lshlrev_b32 v61, 2, v21\n"
    "ds_read_b32 v148, v61 offset:13568\n"
    "ds_read_b32 v150, v61 offset:13632\n"
    "ds_read_b32 v152, v61 offset:13696\n"
    "ds_read_b32 v154, v61 offset:13760\n"
    "s_lshl_b32 s81, s59, 13\n"
    "s_lshl_b32 s75, s61, 7\n"
    "s_add_u32 s81, s81, s75\n"
    "s_lshl_b32 s82, s59, 10\n"
    "s_add_u32 s82, s82, 0x18000\n"
    "s_lshr_b32 s75, s59, 1\n"
    "s_and_b32 s76, s59, 1\n"
    "s_mul_i32 s83, s75, 0x15800\n"
    "s_mul_i32 s77, s76, 0x5600\n"
    "s_add_u32 s83, s83, s77\n"
    "s_lshl_b32 s77, s64, 6\n"
    "s_add_u32 s83, s83, s77\n"
    "s_mul_i32 s84, s59, 0xc00\n"
    "s_lshl_b32 s76, s76, 5\n"
    "s_mul_i32 s75, s75, 0xc000\n"
    "v_xor_b32 v232, s76, v4\n"
    "v_add_u32 v232, s75, v232\n"
    "s_lshl_b32 s87, s57, 7\n"
    "s_lshl_b32 s75, s60, 6\n"
    "s_add_u32 s87, s87, s75\n"
    "s_lshl_b32 s75, s59, 4\n"
    "s_add_u32 s87, s87, s75\n"
    "s_lshl_b32 s85, s87, 10\n"
    "s_lshl_b32 s75, s61, 7\n"
    "s_add_u32 s85, s85, s75\n"
    "s_lshl_b32 s86, s59, 11\n"
    "s_add_u32 s86, s86, 0x3800\n"
    "s_lshl_b32 s75, s59, 11\n"
    "v_add_u32 v62, s75, v1\n"
    "s_lshl_b32 s75, s59, 7\n"
    "v_lshl_add_u32 v63, v22, 4, s75\n"
    "s_waitcnt lgkmcnt(3)\n"
    "v_pk_fma_f32 v[112:113], v[168:169], v[148:149], v[112:113] op_sel_hi:[1,0,1] neg_lo:[1,0,0] neg_hi:[1,0,0]\n"
    "v_pk_fma_f32 v[114:115], v[170:171], v[148:149], v[114:115] op_sel_hi:[1,0,1] neg_lo:[1,0,0] neg_hi:[1,0,0]\n"
    "v_pk_fma_f32 v[120:121], v[172:173], v[148:149], v[120:121] op_sel_hi:[1,0,1] neg_lo:[1,0,0] neg_hi:[1,0,0]\n"
    "v_pk_fma_f32 v[122:123], v[174:175], v[148:149], v[122:123] op_sel_hi:[1,0,1] neg_lo:[1,0,0] neg_hi:[1,0,0]\n"
    "v_pk_mul_f32 v[212:213], v[112:113], v[184:185]\n"
    "v_pk_mul_f32 v[214:215], v[114:115], v[186:187]\n"
    "v_pk_mul_f32 v[216:217], v[120:121], v[188:189]\n"
    "v_pk_mul_f32 v[218:219], v[122:123], v[190:191]\n"
    "v_pk_fma_f32 v[112:113], v[112:113], v[176:177], v[192:193]\n"
    "v_pk_fma_f32 v[114:115], v[114:115], v[178:179], v[192:193]\n"
    "v_pk_fma_f32 v[120:121], v[120:121], v[180:181], v[192:193]\n"
    "v_pk_fma_f32 v[122:123], v[122:123], v[182:183], v[192:193]\n"
    "v_exp_f32 v212, v212 clamp\n"
    "v_exp_f32 v213, v213 clamp\n"
    "v_exp_f32 v214, v214 clamp\n"
    "v_exp_f32 v215, v215 clamp\n"
    "v_exp_f32 v216, v216 clamp\n"
    "v_exp_f32 v217, v217 clamp\n"
    "v_exp_f32 v218, v218 clamp\n"
    "v_exp_f32 v219, v219 clamp\n"
    "v_max_f32 v112, v192, v112\n"
    "v_max_f32 v113, v192, v113\n"
    "v_max_f32 v114, v192, v114\n"
    "v_max_f32 v115, v192, v115\n"
    "v_max_f32 v120, v192, v120\n"
    "v_max_f32 v121, v192, v121\n"
    "v_max_f32 v122, v192, v122\n"
    "v_max_f32 v123, v192, v123\n"
    "v_pk_add_f32 v[112:113], v[112:113], v[212:213]\n"
    "v_pk_add_f32 v[114:115], v[114:115], v[214:215]\n"
    "v_pk_add_f32 v[120:121], v[120:121], v[216:217]\n"
    "v_pk_add_f32 v[122:123], v[122:123], v[218:219]\n"
    "v_cvt_pk_bf16_f32 v196, v112, v113\n"
    "v_cvt_pk_bf16_f32 v197, v114, v115\n"
    "v_cvt_pk_bf16_f32 v198, v120, v121\n"
    "v_cvt_pk_bf16_f32 v199, v122, v123\n"
    "s_nop 1\n"
    "v_mfma_f32_16x16x32_bf16 v[236:239], v[196:199], v[24:27], 0\n"
    "s_waitcnt lgkmcnt(1)\n"
    "v_pk_fma_f32 v[128:129], v[168:169], v[152:153], v[128:129] op_sel_hi:[1,0,1] neg_lo:[1,0,0] neg_hi:[1,0,0]\n"
    "v_pk_fma_f32 v[130:131], v[170:171], v[152:153], v[130:131] op_sel_hi:[1,0,1] neg_lo:[1,0,0] neg_hi:[1,0,0]\n"
    "v_pk_fma_f32 v[136:137], v[172:173], v[152:153], v[136:137] op_sel_hi:[1,0,1] neg_lo:[1,0,0] neg_hi:[1,0,0]\n"
    "v_pk_fma_f32 v[138:139], v[174:175], v[152:153], v[138:139] op_sel_hi:[1,0,1] neg_lo:[1,0,0] neg_hi:[1,0,0]\n"
    "v_pk_mul_f32 v[128:129], v[128:129], v[176:177]\n"
    "v_pk_mul_f32 v[130:131], v[130:131], v[178:179]\n"
    "v_pk_mul_f32 v[136:137], v[136:137], v[180:181]\n"
    "v_pk_mul_f32 v[138:139], v[138:139], v[182:183]\n"
    "v_cvt_pk_bf16_f32 v204, v128, v129\n"
    "v_cvt_pk_bf16_f32 v205, v130, v131\n"
    "v_cvt_pk_bf16_f32 v206, v136, v137\n"
    "v_cvt_pk_bf16_f32 v207, v138, v139\n"
    "s_nop 1\n"
    "v_mfma_f32_16x16x32_bf16 v[220:223], v[196:199], v[204:207], 0\n"
    "v_pk_fma_f32 v[116:117], v[168:169], v[150:151], v[116:117] op_sel_hi:[1,0,1] neg_lo:[1,0,0] neg_hi:[1,0,0]\n"
    "v_pk_fma_f32 v[118:119], v[170:171], v[150:151], v[118:119] op_sel_hi:[1,0,1] neg_lo:[1,0,0] neg_hi:[1,0,0]\n"
    "v_pk_fma_f32 v[124:125], v[172:173], v[150:151], v[124:125] op_sel_hi:[1,0,1] neg_lo:[1,0,0] neg_hi:[1,0,0]\n"
    "v_pk_fma_f32 v[126:127], v[174:175], v[150:151], v[126:127] op_sel_hi:[1,0,1] neg_lo:[1,0,0] neg_hi:[1,0,0]\n"
    "v_pk_mul_f32 v[212:213], v[116:117], v[184:185]\n"
    "v_pk_mul_f32 v[214:215], v[118:119], v[186:187]\n"
    "v_pk_mul_f32 v[216:217], v[124:125], v[188:189]\n"
    "v_pk_mul_f32 v[218:219], v[126:127], v[190:191]\n"
    "v_pk_fma_f32 v[116:117], v[116:117], v[176:177], v[192:193]\n"
    "v_pk_fma_f32 v[118:119], v[118:119], v[178:179], v[192:193]\n"
    "v_pk_fma_f32 v[124:125], v[124:125], v[180:181], v[192:193]\n"
    "v_pk_fma_f32 v[126:127], v[126:127], v[182:183], v[192:193]\n"
    "v_exp_f32 v212, v212 clamp\n"
    "v_exp_f32 v213, v213 clamp\n"
    "v_exp_f32 v214, v214 clamp\n"
    "v_exp_f32 v215, v215 clamp\n"
    "v_exp_f32 v216, v216 clamp\n"
    "v_exp_f32 v217, v217 clamp\n"
    "v_exp_f32 v218, v218 clamp\n"
    "v_exp_f32 v219, v219 clamp\n"
    "v_max_f32 v116, v192, v116\n"
    "v_max_f32 v117, v192, v117\n"
    "v_max_f32 v118, v192, v118\n"
    "v_max_f32 v119, v192, v119\n"
    "v_max_f32 v124, v192, v124\n"
    "v_max_f32 v125, v192, v125\n"
    "v_max_f32 v126, v192, v126\n"
    "v_max_f32 v127, v192, v127\n"
    "v_pk_add_f32 v[116:117], v[116:117], v[212:213]\n"
    "v_pk_add_f32 v[118:119], v[118:119], v[214:215]\n"
    "v_pk_add_f32 v[124:125], v[124:125], v[216:217]\n"
    "v_pk_add_f32 v[126:127], v[126:127], v[218:219]\n"
    "v_cvt_pk_bf16_f32 v200, v116, v117\n"
    "v_cvt_pk_bf16_f32 v201, v118, v119\n"
    "v_cvt_pk_bf16_f32 v202, v124, v125\n"
    "v_cvt_pk_bf16_f32 v203, v126, v127\n"
    "ds_write_b128 v62, v[220:223]\n"
    "ds_write_b128 v63, v[236:239] offset:8192\n"
    "v_mfma_f32_16x16x32_bf16 v[240:243], v[200:203], v[24:27], 0\n"
    "s_waitcnt lgkmcnt(2)\n"
    "v_pk_fma_f32 v[132:133], v[168:169], v[154:155], v[132:133] op_sel_hi:[1,0,1] neg_lo:[1,0,0] neg_hi:[1,0,0]\n"
    "v_pk_fma_f32 v[134:135], v[170:171], v[154:155], v[134:135] op_sel_hi:[1,0,1] neg_lo:[1,0,0] neg_hi:[1,0,0]\n"
    "v_pk_fma_f32 v[140:141], v[172:173], v[154:155], v[140:141] op_sel_hi:[1,0,1] neg_lo:[1,0,0] neg_hi:[1,0,0]\n"
    "v_pk_fma_f32 v[142:143], v[174:175], v[154:155], v[142:143] op_sel_hi:[1,0,1] neg_lo:[1,0,0] neg_hi:[1,0,0]\n"
    "v_pk_mul_f32 v[132:133], v[132:133], v[176:177]\n"
    "v_pk_mul_f32 v[134:135], v[134:135], v[178:179]\n"
    "v_pk_mul_f32 v[140:141], v[140:141], v[180:181]\n"
    "v_pk_mul_f32 v[142:143], v[142:143], v[182:183]\n"
    "v_cvt_pk_bf16_f32 v208, v132, v133\n"
    "v_cvt_pk_bf16_f32 v209, v134, v135\n"
    "v_cvt_pk_bf16_f32 v210, v140, v141\n"
    "v_cvt_pk_bf16_f32 v211, v142, v143\n"
    "s_nop 1\n"
    "v_mfma_f32_16x16x32_bf16 v[224:227], v[200:203], v[208:211], 0\n"
    "ds_write_b128 v63, v[240:243] offset:8256\n"
    "s_nop 6\n"
    "ds_write_b128 v62, v[224:227] offset:1024\n"
    "s_mul_i32 s80, s58, 0x9000\n"
    "s_add_u32 s80, s80, 0x40000\n"
    "s_waitcnt vmcnt(1)\n"
    "s_waitcnt lgkmcnt(0)\n"
    "s_barrier\n"
    "s_cmp_gt_u32 s59, 1\n"
    "s_cbranch_scc1 .Lo4_%=\n"
    "s_lshl_b32 s75, s59, 10\n"
    "v_add_u32 v61, s75, v1\n"
    "s_lshl_b32 s75, s59, 6\n"
    "v_lshl_add_u32 v63, v22, 4, s75\n"
    "ds_read_b128 v[196:199], v61\n"
    "ds_read_b128 v[200:203], v61 offset:2048\n"
    "ds_read_b128 v[204:207], v61 offset:4096\n"
    "ds_read_b128 v[208:211], v61 offset:6144\n"
    "ds_read_b128 v[212:215], v63 offset:8192\n"
    "ds_read_b128 v[216:219], v63 offset:8320\n"
    "ds_read_b128 v[220:223], v63 offset:8448\n"
    "ds_read_b128 v[224:227], v63 offset:8576\n"
    "s_mul_i32 s75, s57, 0x900\n"
    "s_add_u32 s75, s75, s80\n"
    "s_waitcnt lgkmcnt(6)\n"
    "v_pk_add_f32 v[196:197], v[196:197], v[200:201]\n"
    "s_waitcnt lgkmcnt(4)\n"
    "v_pk_add_f32 v[204:205], v[204:205], v[208:209]\n"
    "v_pk_add_f32 v[198:199], v[198:199], v[202:203]\n"
    "v_pk_add_f32 v[206:207], v[206:207], v[210:211]\n"
    "v_pk_add_f32 v[196:197], v[196:197], v[204:205]\n"
    "v_pk_add_f32 v[198:199], v[198:199], v[206:207]\n"
    "v_cvt_pk_bf16_f32 v200, v196, v197\n"
    "v_cvt_pk_bf16_f32 v202, v198, v199\n"
    "v_mov_b32 v201, s62\n"
    "v_mov_b32 v203, s62\n"
    "buffer_store_dwordx4 v[200:203], v61, s[52:55], s75 offen sc1\n"
    "s_waitcnt lgkmcnt(2)\n"
    "v_pk_add_f32 v[212:213], v[212:213], v[216:217]\n"
    "s_waitcnt lgkmcnt(0)\n"
    "v_pk_add_f32 v[220:221], v[220:221], v[224:225]\n"
    "v_pk_add_f32 v[214:215], v[214:215], v[218:219]\n"
    "v_pk_add_f32 v[222:223], v[222:223], v[226:227]\n"
    "v_pk_add_f32 v[212:213], v[212:213], v[220:221]\n"
    "v_pk_add_f32 v[214:215], v[214:215], v[222:223]\n"
    "v_cvt_pk_bf16_f32 v216, v212, v213\n"
    "v_cvt_pk_bf16_f32 v218, v214, v215\n"
    "v_mov_b32 v217, s62\n"
    "v_mov_b32 v219, s62\n"
    "v_lshl_add_u32 v63, v21, 6, v63\n"
    "s_lshl_b32 s76, s59, 6\n"
    "v_add_u32 v63, s76, v63\n"
    "s_mov_b32 exec_lo, 0x30003\n"
    "s_mov_b32 exec_hi, 0x30003\n"
    "buffer_store_dwordx4 v[216:219], v63, s[52:55], s75 offen offset:2048 sc1\n"
    "s_mov_b64 exec, -1\n"
    ".Lo4_%=:\n"
    "s_lshl_b32 s75, s59, 18\n"
    "s_lshl2_add_u32 s75, s18, s75\n"
    "s_lshl_b32 s76, s64, 9\n"
    "s_add_u32 s75, s75, s76\n"
    "s_add_u32 s13, s75, 0x1800000\n"
    "s_lshr_b32 s75, s59, 1\n"
    "s_mul_i32 s88, s75, 0x4800\n"
    "s_add_u32 s88, s88, s80\n"
    "s_and_b32 s76, s59, 1\n"
    "v_bfe_u32 v61, v23, 2, 3\n"
    "v_mul_u32_u24 v61, 0x900, v61\n"
    "v_and_b32 v62, 3, v23\n"
    "v_lshl_add_u32 v61, v62, 4, v61\n"
    "s_lshl_b32 s77, s76, 7\n"
    "v_add_u32 v244, s77, v61\n"
    "s_lshl_b32 s77, s76, 10\n"
    "v_add_u32 v245, s77, v1\n"
    "ds_read_b64_tr_b16 v[64:65], v4 offset:24576\n"
    "ds_read_b64_tr_b16 v[66:67], v4 offset:49152\n"
    "ds_read_b64_tr_b16 v[68:69], v5 offset:24576\n"
    "ds_read_b64_tr_b16 v[70:71], v5 offset:49152\n"
    "ds_read_b64_tr_b16 v[72:73], v232 offset:24576\n"
    "ds_read_b64_tr_b16 v[74:75], v232 offset:49152\n"
    "ds_read_b64_tr_b16 v[76:77], v246 offset:24576\n"
    "ds_read_b64_tr_b16 v[78:79], v246 offset:49152\n"
    "ds_read_b64_tr_b16 v[80:81], v247 offset:24576\n"
    "ds_read_b64_tr_b16 v[82:83], v247 offset:49152\n"
    "ds_read_b64_tr_b16 v[84:85], v232 offset:25600\n"
    "ds_read_b64_tr_b16 v[86:87], v232 offset:50176\n"
    "s_waitcnt lgkmcnt(10)\n"
    "v_mfma_f32_16x16x32_bf16 v[112:115], v[64:67], a[0:3], 0\n"
    "ds_read_b64_tr_b16 v[88:89], v4 offset:25600\n"
    "ds_read_b64_tr_b16 v[90:91], v4 offset:50176\n"
    "v_mfma_f32_16x16x32_bf16 v[116:119], v[64:67], a[64:67], 0\n"
    "ds_read_b64_tr_b16 v[92:93], v5 offset:25600\n"
    "s_add_u32 m0, s86, 0\n"
    "s_nop 0\n"
    "buffer_load_dwordx4 v16, s[24:27], s85 offen lds\n"
    "s_waitcnt lgkmcnt(11)\n"
    "v_mfma_f32_16x16x32_bf16 v[120:123], v[68:71], a[0:3], 0\n"
    "ds_read_b64_tr_b16 v[94:95], v5 offset:50176\n"
    "v_mfma_f32_16x16x32_bf16 v[124:127], v[68:71], a[64:67], 0\n"
    "ds_read_b64_tr_b16 v[96:97], v232 offset:26624\n"
    "s_add_u32 s67, s85, 0x1c00\n"
    "buffer_load_dwordx4 v17, s[24:27], s67 offen offset:1024 lds\n"
    "s_waitcnt lgkmcnt(11)\n"
    "v_mfma_f32_16x16x32_bf16 v[144:147], v[72:75], v[24:27], 0\n"
    "ds_read_b64_tr_b16 v[98:99], v232 offset:51200\n"
    "s_waitcnt lgkmcnt(10)\n"
    "v_mfma_f32_16x16x32_bf16 v[112:115], v[76:79], a[4:7], v[112:115]\n"
    "ds_read_b64_tr_b16 v[100:101], v246 offset:25600\n"
    "ds_read_b64_tr_b16 v[102:103], v246 offset:50176\n"
    "v_mfma_f32_16x16x32_bf16 v[116:119], v[76:79], a[68:71], v[116:119]\n"
    "ds_read_b64_tr_b16 v[104:105], v247 offset:25600\n"
    "s_add_u32 m0, s82, 0x2000\n"
    "s_add_u32 s65, s81, 0x10000\n"
    "buffer_load_dwordx4 v14, s[32:35], s65 offen lds\n"
    "s_waitcnt lgkmcnt(11)\n"
    "v_mfma_f32_16x16x32_bf16 v[120:123], v[80:83], a[4:7], v[120:123]\n"
    "ds_read_b64_tr_b16 v[106:107], v247 offset:50176\n"
    "v_mfma_f32_16x16x32_bf16 v[124:127], v[80:83], a[68:71], v[124:127]\n"
    "ds_read_b64_tr_b16 v[108:109], v232 offset:27648\n"
    "s_addk_i32 m0, 4096\n"
    "s_add_u32 s65, s81, 0x18000\n"
    "buffer_load_dwordx4 v14, s[32:35], s65 offen lds\n"
    "s_waitcnt lgkmcnt(11)\n"
    "v_mfma_f32_16x16x32_bf16 v[144:147], v[84:87], v[24:27], v[144:147]\n"
    "ds_read_b64_tr_b16 v[110:111], v232 offset:52224\n"
    "s_waitcnt lgkmcnt(10)\n"
    "v_mfma_f32_16x16x32_bf16 v[112:115], v[88:91], a[8:11], v[112:115]\n"
    "ds_read_b64_tr_b16 v[64:65], v4 offset:26624\n"
    "ds_read_b64_tr_b16 v[66:67], v4 offset:51200\n"
    "v_mfma_f32_16x16x32_bf16 v[116:119], v[88:91], a[72:75], v[116:119]\n"
    "ds_read_b64_tr_b16 v[68:69], v5 offset:26624\n"
    "s_waitcnt lgkmcnt(11)\n"
    "v_mfma_f32_16x16x32_bf16 v[120:123], v[92:95], a[8:11], v[120:123]\n"
    "ds_read_b64_tr_b16 v[70:71], v5 offset:51200\n"
    "s_addk_i32 m0, 4096\n"
    "s_add_u32 s65, s81, 0x20000\n"
    "buffer_load_dwordx4 v14, s[32:35], s65 offen lds\n"
    "v_mfma_f32_16x16x32_bf16 v[124:127], v[92:95], a[72:75], v[124:127]\n"
    "ds_read_b64_tr_b16 v[72:73], v232 offset:28672\n"
    "s_waitcnt lgkmcnt(11)\n"
    "v_mfma_f32_16x16x32_bf16 v[144:147], v[96:99], v[24:27], v[144:147]\n"
    "ds_read_b64_tr_b16 v[74:75], v232 offset:53248\n"
    "s_waitcnt lgkmcnt(10)\n"
    "v_mfma_f32_16x16x32_bf16 v[112:115], v[100:103], a[12:15], v[112:115]\n"
    "ds_read_b64_tr_b16 v[76:77], v246 offset:26624\n"
    "ds_read_b64_tr_b16 v[78:79], v246 offset:51200\n"
    "v_mfma_f32_16x16x32_bf16 v[116:119], v[100:103], a[76:79], v[116:119]\n"
    "ds_read_b64_tr_b16 v[80:81], v247 offset:26624\n"
    "s_waitcnt lgkmcnt(11)\n"
    "v_mfma_f32_16x16x32_bf16 v[120:123], v[104:107], a[12:15], v[120:123]\n"
    "ds_read_b64_tr_b16 v[82:83], v247 offset:51200\n"
    "s_addk_i32 m0, 4096\n"
    "s_add_u32 s65, s81, 0x28000\n"
    "buffer_load_dwordx4 v14, s[32:35], s65 offen lds\n"
    "v_mfma_f32_16x16x32_bf16 v[124:127], v[104:107], a[76:79], v[124:127]\n"
    "ds_read_b64_tr_b16 v[84:85], v232 offset:29696\n"
    "s_waitcnt lgkmcnt(11)\n"
    "v_mfma_f32_16x16x32_bf16 v[144:147], v[108:111], v[24:27], v[144:147]\n"
    "ds_read_b64_tr_b16 v[86:87], v232 offset:54272\n"
    "s_waitcnt lgkmcnt(10)\n"
    "v_mfma_f32_16x16x32_bf16 v[112:115], v[64:67], a[16:19], v[112:115]\n"
    "ds_read_b64_tr_b16 v[88:89], v4 offset:27648\n"
    "ds_read_b64_tr_b16 v[90:91], v4 offset:52224\n"
    "v_mfma_f32_16x16x32_bf16 v[116:119], v[64:67], a[80:83], v[116:119]\n"
    "ds_read_b64_tr_b16 v[92:93], v5 offset:27648\n"
    "s_waitcnt lgkmcnt(11)\n"
    "v_mfma_f32_16x16x32_bf16 v[120:123], v[68:71], a[16:19], v[120:123]\n"
    "ds_read_b64_tr_b16 v[94:95], v5 offset:52224\n"
    "s_addk_i32 m0, 12288\n"
    "s_add_u32 s65, s81, 0x40000\n"
    "buffer_load_dwordx4 v14, s[32:35], s65 offen lds\n"
    "v_mfma_f32_16x16x32_bf16 v[124:127], v[68:71], a[80:83], v[124:127]\n"
    "ds_read_b64_tr_b16 v[96:97], v232 offset:30720\n"
    "s_waitcnt lgkmcnt(11)\n"
    "v_mfma_f32_16x16x32_bf16 v[144:147], v[72:75], v[24:27], v[144:147]\n"
    "ds_read_b64_tr_b16 v[98:99], v232 offset:55296\n"
    "s_waitcnt lgkmcnt(10)\n"
    "v_mfma_f32_16x16x32_bf16 v[112:115], v[76:79], a[20:23], v[112:115]\n"
    "ds_read_b64_tr_b16 v[100:101], v246 offset:27648\n"
    "ds_read_b64_tr_b16 v[102:103], v246 offset:52224\n"
    "v_mfma_f32_16x16x32_bf16 v[116:119], v[76:79], a[84:87], v[116:119]\n"
    "ds_read_b64_tr_b16 v[104:105], v247 offset:27648\n"
    "s_waitcnt lgkmcnt(11)\n"
    "v_mfma_f32_16x16x32_bf16 v[120:123], v[80:83], a[20:23], v[120:123]\n"
    "ds_read_b64_tr_b16 v[106:107], v247 offset:52224\n"
    "s_addk_i32 m0, 4096\n"
    "s_add_u32 s65, s81, 0x48000\n"
    "buffer_load_dwordx4 v14, s[32:35], s65 offen lds\n"
    "v_mfma_f32_16x16x32_bf16 v[124:127], v[80:83], a[84:87], v[124:127]\n"
    "ds_read_b64_tr_b16 v[108:109], v232 offset:31744\n"
    "s_waitcnt lgkmcnt(11)\n"
    "v_mfma_f32_16x16x32_bf16 v[144:147], v[84:87], v[24:27], v[144:147]\n"
    "ds_read_b64_tr_b16 v[110:111], v232 offset:56320\n"
    "s_waitcnt lgkmcnt(10)\n"
    "v_mfma_f32_16x16x32_bf16 v[112:115], v[88:91], a[24:27], v[112:115]\n"
    "ds_read_b64_tr_b16 v[64:65], v4 offset:28672\n"
    "v_mfma_f32_16x16x32_bf16 v[116:119], v[88:91], a[88:91], v[116:119]\n"
    "ds_read_b64_tr_b16 v[66:67], v4 offset:53248\n"
    "s_waitcnt lgkmcnt(10)\n"
    "v_mfma_f32_16x16x32_bf16 v[120:123], v[92:95], a[24:27], v[120:123]\n"
    "ds_read_b64_tr_b16 v[68:69], v5 offset:28672\n"
    "s_addk_i32 m0, 4096\n"
    "s_add_u32 s65, s81, 0x50000\n"
    "buffer_load_dwordx4 v14, s[32:35], s65 offen lds\n"
    "v_mfma_f32_16x16x32_bf16 v[124:127], v[92:95], a[88:91], v[124:127]\n"
    "ds_read_b64_tr_b16 v[70:71], v5 offset:53248\n"
    "s_waitcnt lgkmcnt(10)\n"
    "v_mfma_f32_16x16x32_bf16 v[144:147], v[96:99], v[24:27], v[144:147]\n"
    "s_waitcnt lgkmcnt(8)\n"
    "v_mfma_f32_16x16x32_bf16 v[112:115], v[100:103], a[28:31], v[112:115]\n"
    "ds_read_b64_tr_b16 v[76:77], v246 offset:28672\n"
    "v_mfma_f32_16x16x32_bf16 v[116:119], v[100:103], a[92:95], v[116:119]\n"
    "ds_read_b64_tr_b16 v[78:79], v246 offset:53248\n"
    "s_waitcnt lgkmcnt(8)\n"
    "v_mfma_f32_16x16x32_bf16 v[120:123], v[104:107], a[28:31], v[120:123]\n"
    "ds_read_b64_tr_b16 v[80:81], v247 offset:28672\n"
    "s_addk_i32 m0, 4096\n"
    "s_add_u32 s65, s81, 0x58000\n"
    "buffer_load_dwordx4 v14, s[32:35], s65 offen lds\n"
    "v_mfma_f32_16x16x32_bf16 v[124:127], v[104:107], a[92:95], v[124:127]\n"
    "ds_read_b64_tr_b16 v[82:83], v247 offset:53248\n"
    "s_waitcnt lgkmcnt(8)\n"
    "v_mfma_f32_16x16x32_bf16 v[144:147], v[108:111], v[24:27], v[144:147]\n"
    "s_waitcnt lgkmcnt(6)\n"
    "v_mfma_f32_16x16x32_bf16 v[112:115], v[64:67], a[32:35], v[112:115]\n"
    "ds_read_b64_tr_b16 v[88:89], v4 offset:29696\n"
    "v_mfma_f32_16x16x32_bf16 v[116:119], v[64:67], a[96:99], v[116:119]\n"
    "ds_read_b64_tr_b16 v[90:91], v4 offset:54272\n"
    "s_waitcnt lgkmcnt(6)\n"
    "v_mfma_f32_16x16x32_bf16 v[120:123], v[68:71], a[32:35], v[120:123]\n"
    "ds_read_b64_tr_b16 v[92:93], v5 offset:29696\n"
    "s_addk_i32 m0, 12288\n"
    "s_add_u32 s65, s81, 0x70000\n"
    "buffer_load_dwordx4 v14, s[32:35], s65 offen lds\n"
    "v_mfma_f32_16x16x32_bf16 v[124:127], v[68:71], a[96:99], v[124:127]\n"
    "ds_read_b64_tr_b16 v[94:95], v5 offset:54272\n"
    "s_waitcnt lgkmcnt(6)\n"
    "v_mfma_f32_16x16x32_bf16 v[112:115], v[76:79], a[36:39], v[112:115]\n"
    "ds_read_b64_tr_b16 v[100:101], v246 offset:29696\n"
    "v_mfma_f32_16x16x32_bf16 v[116:119], v[76:79], a[100:103], v[116:119]\n"
    "ds_read_b64_tr_b16 v[102:103], v246 offset:54272\n"
    "s_waitcnt lgkmcnt(6)\n"
    "v_mfma_f32_16x16x32_bf16 v[120:123], v[80:83], a[36:39], v[120:123]\n"
    "ds_read_b64_tr_b16 v[104:105], v247 offset:29696\n"
    "s_addk_i32 m0, 4096\n"
    "s_add_u32 s65, s81, 0x78000\n"
    "buffer_load_dwordx4 v14, s[32:35], s65 offen lds\n"
    "v_mfma_f32_16x16x32_bf16 v[124:127], v[80:83], a[100:103], v[124:127]\n"
    "ds_read_b64_tr_b16 v[106:107], v247 offset:54272\n"
    "s_lshl_b32 s75, s59, 6\n"
    "v_lshl_add_u32 v61, v22, 4, s75\n"
    "ds_write_b128 v61, v[144:147] offset:13824\n"
    "s_waitcnt lgkmcnt(7)\n"
    "v_mfma_f32_16x16x32_bf16 v[112:115], v[88:91], a[40:43], v[112:115]\n"
    "ds_read_b64_tr_b16 v[64:65], v4 offset:30720\n"
    "v_mfma_f32_16x16x32_bf16 v[116:119], v[88:91], a[104:107], v[116:119]\n"
    "ds_read_b64_tr_b16 v[66:67], v4 offset:55296\n"
    "s_waitcnt lgkmcnt(7)\n"
    "v_mfma_f32_16x16x32_bf16 v[120:123], v[92:95], a[40:43], v[120:123]\n"
    "ds_read_b64_tr_b16 v[68:69], v5 offset:30720\n"
    "v_mfma_f32_16x16x32_bf16 v[124:127], v[92:95], a[104:107], v[124:127]\n"
    "ds_read_b64_tr_b16 v[70:71], v5 offset:55296\n"
    "s_waitcnt lgkmcnt(7)\n"
    "v_mfma_f32_16x16x32_bf16 v[112:115], v[100:103], a[44:47], v[112:115]\n"
    "ds_read_b64_tr_b16 v[76:77], v246 offset:30720\n"
    "v_mfma_f32_16x16x32_bf16 v[116:119], v[100:103], a[108:111], v[116:119]\n"
    "ds_read_b64_tr_b16 v[78:79], v246 offset:55296\n"
    "s_waitcnt lgkmcnt(7)\n"
    "v_mfma_f32_16x16x32_bf16 v[120:123], v[104:107], a[44:47], v[120:123]\n"
    "ds_read_b64_tr_b16 v[80:81], v247 offset:30720\n"
    "v_mfma_f32_16x16x32_bf16 v[124:127], v[104:107], a[108:111], v[124:127]\n"
    "ds_read_b64_tr_b16 v[82:83], v247 offset:55296\n"
    "s_waitcnt lgkmcnt(6)\n"
    "v_mfma_f32_16x16x32_bf16 v[112:115], v[64:67], a[48:51], v[112:115]\n"
    "ds_read_b64_tr_b16 v[88:89], v4 offset:31744\n"
    "v_mfma_f32_16x16x32_bf16 v[116:119], v[64:67], a[112:115], v[116:119]\n"
    "ds_read_b64_tr_b16 v[90:91], v4 offset:56320\n"
    "s_waitcnt lgkmcnt(6)\n"
    "v_mfma_f32_16x16x32_bf16 v[120:123], v[68:71], a[48:51], v[120:123]\n"
    "ds_read_b64_tr_b16 v[92:93], v5 offset:31744\n"
    "buffer_load_dwordx4 v[200:203], v245, s[52:55], s88 offen sc1\n"
    "v_mfma_f32_16x16x32_bf16 v[124:127], v[68:71], a[112:115], v[124:127]\n"
    "ds_read_b64_tr_b16 v[94:95], v5 offset:56320\n"
    "s_barrier\n"
    "v_lshlrev_b32 v61, 4, v22\n"
    "ds_read_b128 v[128:131], v61 offset:13824\n"
    "ds_read_b128 v[132:135], v61 offset:13888\n"
    "ds_read_b128 v[136:139], v61 offset:13952\n"
    "ds_read_b128 v[140:143], v61 offset:14016\n"
    "s_waitcnt lgkmcnt(10)\n"
    "v_mfma_f32_16x16x32_bf16 v[112:115], v[76:79], a[52:55], v[112:115]\n"
    "ds_read_b64_tr_b16 v[100:101], v246 offset:31744\n"
    "v_mfma_f32_16x16x32_bf16 v[116:119], v[76:79], a[116:119], v[116:119]\n"
    "ds_read_b64_tr_b16 v[102:103], v246 offset:56320\n"
    "buffer_load_dwordx4 v[204:207], v245, s[52:55], s88 offen offset:2304 sc1\n"
    "s_waitcnt lgkmcnt(10)\n"
    "v_mfma_f32_16x16x32_bf16 v[120:123], v[80:83], a[52:55], v[120:123]\n"
    "ds_read_b64_tr_b16 v[104:105], v247 offset:31744\n"
    "v_mfma_f32_16x16x32_bf16 v[124:127], v[80:83], a[116:119], v[124:127]\n"
    "ds_read_b64_tr_b16 v[106:107], v247 offset:56320\n"
    "s_add_u32 s68, s88, 0x1200\n"
    "buffer_load_dwordx4 v[208:211], v245, s[52:55], s68 offen sc1\n"
    "s_waitcnt lgkmcnt(10)\n"
    "v_mfma_f32_16x16x32_bf16 v[112:115], v[88:91], a[56:59], v[112:115]\n"
    "v_mfma_f32_16x16x32_bf16 v[116:119], v[88:91], a[120:123], v[116:119]\n"
    "buffer_load_dwordx4 v[212:215], v245, s[52:55], s68 offen offset:2304 sc1\n"
    "s_waitcnt lgkmcnt(8)\n"
    "v_mfma_f32_16x16x32_bf16 v[120:123], v[92:95], a[56:59], v[120:123]\n"
    "v_mfma_f32_16x16x32_bf16 v[124:127], v[92:95], a[120:123], v[124:127]\n"
    "s_addk_i32 s68, 4608\n"
    "buffer_load_dwordx4 v[216:219], v245, s[52:55], s68 offen sc1\n"
    "s_waitcnt lgkmcnt(2)\n"
    "v_mfma_f32_16x16x32_bf16 v[112:115], v[100:103], a[60:63], v[112:115]\n"
    "v_mfma_f32_16x16x32_bf16 v[116:119], v[100:103], a[124:127], v[116:119]\n"
    "buffer_load_dwordx4 v[220:223], v245, s[52:55], s68 offen offset:2304 sc1\n"
    "s_waitcnt lgkmcnt(0)\n"
    "v_mfma_f32_16x16x32_bf16 v[120:123], v[104:107], a[60:63], v[120:123]\n"
    "v_mfma_f32_16x16x32_bf16 v[124:127], v[104:107], a[124:127], v[124:127]\n"
    "s_addk_i32 s68, 4608\n"
    "buffer_load_dwordx4 v[224:227], v245, s[52:55], s68 offen sc1\n"
    "buffer_load_dwordx4 v[228:231], v245, s[52:55], s68 offen offset:2304 sc1\n"
    "buffer_load_dwordx4 v[196:199], v244, s[52:55], s88 offen offset:2048 sc1\n"
    "v_pk_add_f32 v[128:129], v[128:129], v[136:137]\n"
    "v_pk_add_f32 v[130:131], v[130:131], v[138:139]\n"
    "v_pk_add_f32 v[132:133], v[132:133], v[140:141]\n"
    "v_pk_add_f32 v[134:135], v[134:135], v[142:143]\n"
    "v_pk_fma_f32 v[112:113], v[156:157], v[128:129], v[112:113] op_sel_hi:[0,1,1] neg_lo:[1,0,0] neg_hi:[1,0,0]\n"
    "v_pk_fma_f32 v[114:115], v[156:157], v[130:131], v[114:115] op_sel_hi:[0,1,1] neg_lo:[1,0,0] neg_hi:[1,0,0]\n"
    "v_pk_fma_f32 v[120:121], v[156:157], v[132:133], v[120:121] op_sel_hi:[0,1,1] neg_lo:[1,0,0] neg_hi:[1,0,0]\n"
    "v_pk_fma_f32 v[122:123], v[156:157], v[134:135], v[122:123] op_sel_hi:[0,1,1] neg_lo:[1,0,0] neg_hi:[1,0,0]\n"
    "v_pk_mul_f32 v[64:65], v[112:113], v[164:165] op_sel_hi:[1,0]\n"
    "v_pk_mul_f32 v[66:67], v[114:115], v[164:165] op_sel_hi:[1,0]\n"
    "v_pk_mul_f32 v[68:69], v[120:121], v[164:165] op_sel_hi:[1,0]\n"
    "v_pk_mul_f32 v[70:71], v[122:123], v[164:165] op_sel_hi:[1,0]\n"
    "v_pk_fma_f32 v[112:113], v[112:113], v[158:159], v[192:193] op_sel_hi:[1,0,1]\n"
    "v_pk_fma_f32 v[114:115], v[114:115], v[158:159], v[192:193] op_sel_hi:[1,0,1]\n"
    "v_pk_fma_f32 v[120:121], v[120:121], v[158:159], v[192:193] op_sel_hi:[1,0,1]\n"
    "v_pk_fma_f32 v[122:123], v[122:123], v[158:159], v[192:193] op_sel_hi:[1,0,1]\n"
    "v_exp_f32 v64, v64 clamp\n"
    "v_exp_f32 v65, v65 clamp\n"
    "v_exp_f32 v66, v66 clamp\n"
    "v_exp_f32 v67, v67 clamp\n"
    "v_exp_f32 v68, v68 clamp\n"
    "v_exp_f32 v69, v69 clamp\n"
    "v_exp_f32 v70, v70 clamp\n"
    "v_exp_f32 v71, v71 clamp\n"
    "v_max_f32 v112, v192, v112\n"
    "v_max_f32 v113, v192, v113\n"
    "v_max_f32 v114, v192, v114\n"
    "v_max_f32 v115, v192, v115\n"
    "v_max_f32 v120, v192, v120\n"
    "v_max_f32 v121, v192, v121\n"
    "v_max_f32 v122, v192, v122\n"
    "v_max_f32 v123, v192, v123\n"
    "v_pk_add_f32 v[112:113], v[112:113], v[64:65]\n"
    "v_pk_add_f32 v[114:115], v[114:115], v[66:67]\n"
    "v_pk_add_f32 v[120:121], v[120:121], v[68:69]\n"
    "v_pk_add_f32 v[122:123], v[122:123], v[70:71]\n"
    "v_cvt_pk_bf16_f32 v44, v112, v113\n"
    "v_cvt_pk_bf16_f32 v45, v114, v115\n"
    "v_cvt_pk_bf16_f32 v48, v120, v121\n"
    "v_cvt_pk_bf16_f32 v49, v122, v123\n"
    "s_add_u32 m0, s84, 0x6000\n"
    "s_add_u32 s66, s83, 0x56000\n"
    "buffer_load_dwordx4 v20, s[36:39], s66 offen lds\n"
    "s_addk_i32 s66, 9984\n"
    "buffer_load_dwordx4 v233, s[36:39], s66 offen offset:1024 lds\n"
    "s_addk_i32 s66, -12032\n"
    "buffer_load_dwordx4 v234, s[36:39], s66 offen offset:2048 lds\n"
    "v_pk_fma_f32 v[116:117], v[160:161], v[128:129], v[116:117] op_sel_hi:[0,1,1] neg_lo:[1,0,0] neg_hi:[1,0,0]\n"
    "v_pk_fma_f32 v[118:119], v[160:161], v[130:131], v[118:119] op_sel_hi:[0,1,1] neg_lo:[1,0,0] neg_hi:[1,0,0]\n"
    "v_pk_fma_f32 v[124:125], v[160:161], v[132:133], v[124:125] op_sel_hi:[0,1,1] neg_lo:[1,0,0] neg_hi:[1,0,0]\n"
    "v_pk_fma_f32 v[126:127], v[160:161], v[134:135], v[126:127] op_sel_hi:[0,1,1] neg_lo:[1,0,0] neg_hi:[1,0,0]\n"
    "v_pk_mul_f32 v[64:65], v[116:117], v[166:167] op_sel_hi:[1,0]\n"
    "v_pk_mul_f32 v[66:67], v[118:119], v[166:167] op_sel_hi:[1,0]\n"
    "v_pk_mul_f32 v[68:69], v[124:125], v[166:167] op_sel_hi:[1,0]\n"
    "v_pk_mul_f32 v[70:71], v[126:127], v[166:167] op_sel_hi:[1,0]\n"
    "v_pk_fma_f32 v[116:117], v[116:117], v[162:163], v[192:193] op_sel_hi:[1,0,1]\n"
    "v_pk_fma_f32 v[118:119], v[118:119], v[162:163], v[192:193] op_sel_hi:[1,0,1]\n"
    "v_pk_fma_f32 v[124:125], v[124:125], v[162:163], v[192:193] op_sel_hi:[1,0,1]\n"
    "v_pk_fma_f32 v[126:127], v[126:127], v[162:163], v[192:193] op_sel_hi:[1,0,1]\n"
    "v_exp_f32 v64, v64 clamp\n"
    "v_exp_f32 v65, v65 clamp\n"
    "v_exp_f32 v66, v66 clamp\n"
    "v_exp_f32 v67, v67 clamp\n"
    "v_exp_f32 v68, v68 clamp\n"
    "v_exp_f32 v69, v69 clamp\n"
    "v_exp_f32 v70, v70 clamp\n"
    "v_exp_f32 v71, v71 clamp\n"
    "v_max_f32 v116, v192, v116\n"
    "v_max_f32 v117, v192, v117\n"
    "v_max_f32 v118, v192, v118\n"
    "v_max_f32 v119, v192, v119\n"
    "v_max_f32 v124, v192, v124\n"
    "v_max_f32 v125, v192, v125\n"
    "v_max_f32 v126, v192, v126\n"
    "v_max_f32 v127, v192, v127\n"
    "v_pk_add_f32 v[116:117], v[116:117], v[64:65]\n"
    "v_pk_add_f32 v[118:119], v[118:119], v[66:67]\n"
    "v_pk_add_f32 v[124:125], v[124:125], v[68:69]\n"
    "v_pk_add_f32 v[126:127], v[126:127], v[70:71]\n"
    "v_cvt_pk_bf16_f32 v46, v116, v117\n"
    "v_cvt_pk_bf16_f32 v47, v118, v119\n"
    "v_cvt_pk_bf16_f32 v50, v124, v125\n"
    "v_cvt_pk_bf16_f32 v51, v126, v127\n"
    "s_addk_i32 m0, 12288\n"
    "s_add_u32 s66, s83, 0x81000\n"
    "buffer_load_dwordx4 v20, s[36:39], s66 offen lds\n"
    "s_addk_i32 s66, 9984\n"
    "buffer_load_dwordx4 v233, s[36:39], s66 offen offset:1024 lds\n"
    "s_addk_i32 s66, -12032\n"
    "buffer_load_dwordx4 v234, s[36:39], s66 offen offset:2048 lds\n"
    "s_mov_b32 s69, 0\n"
    "s_waitcnt vmcnt(6)\n"
    "v_bitop3_b32 v64, v197, v199, s62 bitop3:0x7e\n"
    "v_bitop3_b32 v65, v201, v203, s62 bitop3:0x7e\n"
    "v_bitop3_b32 v66, v205, v207, s62 bitop3:0x7e\n"
    "v_bitop3_b32 v67, v209, v211, s62 bitop3:0x7e\n"
    "v_bitop3_b32 v68, v213, v215, s62 bitop3:0x7e\n"
    "v_bitop3_b32 v69, v217, v219, s62 bitop3:0x7e\n"
    "v_bitop3_b32 v70, v221, v223, s62 bitop3:0x7e\n"
    "v_bitop3_b32 v71, v225, v227, s62 bitop3:0x7e\n"
    "v_bitop3_b32 v72, v229, v231, s62 bitop3:0x7e\n"
    "v_or3_b32 v64, v64, v65, v66\n"
    "v_or3_b32 v67, v67, v68, v69\n"
    "v_or3_b32 v70, v70, v71, v72\n"
    "v_or3_b32 v64, v64, v67, v70\n"
    "v_cmp_ne_u32 vcc, 0, v64\n"
    "s_nop 0\n"
    "s_cmp_eq_u64 vcc, 0\n"
    "s_cbranch_scc1 .Ld7_%=\n"
    ".Lp5_%=:\n"
    "buffer_load_dwordx4 v[200:203], v245, s[52:55], s88 offen sc1\n"
    "buffer_load_dwordx4 v[204:207], v245, s[52:55], s88 offen offset:2304 sc1\n"
    "s_add_u32 s68, s88, 0x1200\n"
    "buffer_load_dwordx4 v[208:211], v245, s[52:55], s68 offen sc1\n"
    "buffer_load_dwordx4 v[212:215], v245, s[52:55], s68 offen offset:2304 sc1\n"
    "s_addk_i32 s68, 4608\n"
    "buffer_load_dwordx4 v[216:219], v245, s[52:55], s68 offen sc1\n"
    "buffer_load_dwordx4 v[220:223], v245, s[52:55], s68 offen offset:2304 sc1\n"
    "s_addk_i32 s68, 4608\n"
    "buffer_load_dwordx4 v[224:227], v245, s[52:55], s68 offen sc1\n"
    "buffer_load_dwordx4 v[228:231], v245, s[52:55], s68 offen offset:2304 sc1\n"
    "buffer_load_dwordx4 v[196:199], v244, s[52:55], s88 offen offset:2048 sc1\n"
    "s_waitcnt vmcnt(0)\n"
    "v_bitop3_b32 v64, v197, v199, s62 bitop3:0x7e\n"
    "v_bitop3_b32 v65, v201, v203, s62 bitop3:0x7e\n"
    "v_bitop3_b32 v66, v205, v207, s62 bitop3:0x7e\n"
    "v_bitop3_b32 v67, v209, v211, s62 bitop3:0x7e\n"
    "v_bitop3_b32 v68, v213, v215, s62 bitop3:0x7e\n"
    "v_bitop3_b32 v69, v217, v219, s62 bitop3:0x7e\n"
    "v_bitop3_b32 v70, v221, v223, s62 bitop3:0x7e\n"
    "v_bitop3_b32 v71, v225, v227, s62 bitop3:0x7e\n"
    "v_bitop3_b32 v72, v229, v231, s62 bitop3:0x7e\n"
    "v_or3_b32 v64, v64, v65, v66\n"
    "v_or3_b32 v67, v67, v68, v69\n"
    "v_or3_b32 v70, v70, v71, v72\n"
    "v_or3_b32 v64, v64, v67, v70\n"
    "v_cmp_ne_u32 vcc, 0, v64\n"
    "s_nop 0\n"
    "s_cmp_eq_u64 vcc, 0\n"
    "s_cbranch_scc1 .Ld7_%=\n"
    "s_add_u32 s69, s69, 1\n"
    "s_cmp_lt_u32 s69, 0x20000\n"
    "s_cbranch_scc1 .Lp5_%=\n"
    "s_nop 7\n"
    ".Ld7_%=:\n"
    "v_and_b32 v203, 0xffff0000, v202\n"
    "v_and_b32 v201, 0xffff0000, v200\n"
    "v_lshlrev_b32 v202, 16, v202\n"
    "v_lshlrev_b32 v200, 16, v200\n"
    "v_and_b32 v207, 0xffff0000, v206\n"
    "v_and_b32 v205, 0xffff0000, v204\n"
    "v_lshlrev_b32 v206, 16, v206\n"
    "v_lshlrev_b32 v204, 16, v204\n"
    "v_and_b32 v211, 0xffff0000, v210\n"
    "v_and_b32 v209, 0xffff0000, v208\n"
    "v_lshlrev_b32 v210, 16, v210\n"
    "v_lshlrev_b32 v208, 16, v208\n"
    "v_and_b32 v215, 0xffff0000, v214\n"
    "v_and_b32 v213, 0xffff0000, v212\n"
    "v_lshlrev_b32 v214, 16, v214\n"
    "v_lshlrev_b32 v212, 16, v212\n"
    "v_and_b32 v219, 0xffff0000, v218\n"
    "v_and_b32 v217, 0xffff0000, v216\n"
    "v_lshlrev_b32 v218, 16, v218\n"
    "v_lshlrev_b32 v216, 16, v216\n"
    "v_and_b32 v223, 0xffff0000, v222\n"
    "v_and_b32 v221, 0xffff0000, v220\n"
    "v_lshlrev_b32 v222, 16, v222\n"
    "v_lshlrev_b32 v220, 16, v220\n"
    "v_and_b32 v227, 0xffff0000, v226\n"
    "v_and_b32 v225, 0xffff0000, v224\n"
    "v_lshlrev_b32 v226, 16, v226\n"
    "v_lshlrev_b32 v224, 16, v224\n"
    "v_and_b32 v231, 0xffff0000, v230\n"
    "v_and_b32 v229, 0xffff0000, v228\n"
    "v_lshlrev_b32 v230, 16, v230\n"
    "v_lshlrev_b32 v228, 16, v228\n"
    "v_and_b32 v199, 0xffff0000, v198\n"
    "v_and_b32 v197, 0xffff0000, v196\n"
    "v_lshlrev_b32 v198, 16, v198\n"
    "v_lshlrev_b32 v196, 16, v196\n"
    "v_pk_add_f32 v[200:201], v[200:201], v[204:205]\n"
    "v_pk_add_f32 v[202:203], v[202:203], v[206:207]\n"
    "v_pk_add_f32 v[208:209], v[208:209], v[212:213]\n"
    "v_pk_add_f32 v[210:211], v[210:211], v[214:215]\n"
    "v_pk_add_f32 v[216:217], v[216:217], v[220:221]\n"
    "v_pk_add_f32 v[218:219], v[218:219], v[222:223]\n"
    "v_pk_add_f32 v[224:225], v[224:225], v[228:229]\n"
    "v_pk_add_f32 v[226:227], v[226:227], v[230:231]\n"
    "v_pk_add_f32 v[200:201], v[200:201], v[208:209]\n"
    "v_pk_add_f32 v[202:203], v[202:203], v[210:211]\n"
    "v_pk_add_f32 v[216:217], v[216:217], v[224:225]\n"
    "v_pk_add_f32 v[218:219], v[218:219], v[226:227]\n"
    "v_pk_add_f32 v[200:201], v[200:201], v[216:217]\n"
    "v_pk_add_f32 v[202:203], v[202:203], v[218:219]\n"
    "s_lshl_b32 s75, s59, 10\n"
    "v_add_u32 v61, s75, v1\n"
    "ds_write_b128 v61, v[200:203] offset:9216\n"
    "v_add_f32_dpp v196, v196, v196 row_ror:4 row_mask:0xf bank_mask:0xf\n"
    "v_add_f32_dpp v197, v197, v197 row_ror:4 row_mask:0xf bank_mask:0xf\n"
    "v_add_f32_dpp v198, v198, v198 row_ror:4 row_mask:0xf bank_mask:0xf\n"
    "v_add_f32_dpp v199, v199, v199 row_ror:4 row_mask:0xf bank_mask:0xf\n"
    "v_add_f32_dpp v196, v196, v196 row_ror:8 row_mask:0xf bank_mask:0xf\n"
    "v_add_f32_dpp v197, v197, v197 row_ror:8 row_mask:0xf bank_mask:0xf\n"
    "v_add_f32_dpp v198, v198, v198 row_ror:8 row_mask:0xf bank_mask:0xf\n"
    "v_add_f32_dpp v199, v199, v199 row_ror:8 row_mask:0xf bank_mask:0xf\n"
    "v_mov_b32 v100, v196\n"
    "v_mov_b32 v101, v197\n"
    "v_mov_b32 v102, v198\n"
    "v_mov_b32 v103, v199\n"
    "v_permlane16_swap_b32 v196, v100\n"
    "v_permlane16_swap_b32 v197, v101\n"
    "v_permlane16_swap_b32 v198, v102\n"
    "v_permlane16_swap_b32 v199, v103\n"
    "v_pk_add_f32 v[196:197], v[196:197], v[100:101]\n"
    "v_pk_add_f32 v[198:199], v[198:199], v[102:103]\n"
    "s_lshl_b32 s75, s59, 6\n"
    "v_and_b32 v62, 3, v23\n"
    "v_lshl_add_u32 v62, v62, 4, s75\n"
    "s_mov_b64 exec, 15\n"
    "ds_write_b128 v62, v[196:199] offset:13312\n"
    "s_mov_b64 exec, -1\n"
    "s_add_u32 m0, s84, 0xc000\n"
    "s_add_u32 s66, s83, 0xac000\n"
    "buffer_load_dwordx4 v20, s[36:39], s66 offen lds\n"
    "s_addk_i32 s66, 9984\n"
    "buffer_load_dwordx4 v233, s[36:39], s66 offen offset:1024 lds\n"
    "s_addk_i32 s66, -12032\n"
    "buffer_load_dwordx4 v234, s[36:39], s66 offen offset:2048 lds\n"
    "s_waitcnt lgkmcnt(0)\n"
    "s_barrier\n"
    "v_lshlrev_b32 v61, 4, v22\n"
    "ds_read_b128 v[80:83], v61 offset:13312\n"
    "ds_read_b128 v[84:87], v61 offset:13376\n"
    "ds_read_b128 v[88:91], v61 offset:13440\n"
    "ds_read_b128 v[92:95], v61 offset:13504\n"
    "ds_read_b128 v[64:67], v1 offset:9216\n"
    "ds_read_b128 v[68:71], v1 offset:10240\n"
    "ds_read_b128 v[72:75], v1 offset:11264\n"
    "ds_read_b128 v[76:79], v1 offset:12288\n"
    "s_lshl_b32 s75, s59, 5\n"
    "s_add_u32 s75, s75, s58\n"
    "s_lshl_b32 s75, s75, 8\n"
    "s_add_u32 s89, s18, s75\n"
    "s_add_u32 s89, s89, 0x1000000\n"
    "v_lshrrev_b32 v62, 3, v23\n"
    "v_and_b32 v63, 7, v23\n"
    "v_lshlrev_b32 v63, 4, v63\n"
    "v_lshl_add_u32 v247, v62, 15, v63\n"
    "s_waitcnt lgkmcnt(5)\n"
    "v_pk_add_f32 v[80:81], v[80:81], v[88:89]\n"
    "v_pk_add_f32 v[82:83], v[82:83], v[90:91]\n"
    "v_cvt_pk_bf16_f32 v100, v80, v81\n"
    "v_cvt_pk_bf16_f32 v101, v82, v83\n"
    "s_waitcnt lgkmcnt(4)\n"
    "v_pk_add_f32 v[84:85], v[84:85], v[92:93]\n"
    "v_pk_add_f32 v[86:87], v[86:87], v[94:95]\n"
    "v_cvt_pk_bf16_f32 v102, v84, v85\n"
    "v_cvt_pk_bf16_f32 v103, v86, v87\n"
    "v_mfma_f32_16x16x16_bf16 v[120:123], v[100:101], v[44:45], 0\n"
    "s_nop 0\n"
    "v_mfma_f32_16x16x16_bf16 v[128:131], v[102:103], v[48:49], 0\n"
    "v_mfma_f32_16x16x16_bf16 v[124:127], v[100:101], v[46:47], 0\n"
    "v_mfma_f32_16x16x16_bf16 v[132:135], v[102:103], v[50:51], 0\n"
    "s_waitcnt lgkmcnt(1)\n"
    "v_pk_add_f32 v[64:65], v[64:65], v[72:73]\n"
    "v_pk_add_f32 v[66:67], v[66:67], v[74:75]\n"
    "v_cvt_pk_bf16_f32 v96, v64, v65\n"
    "v_cvt_pk_bf16_f32 v97, v66, v67\n"
    "s_waitcnt lgkmcnt(0)\n"
    "v_pk_add_f32 v[68:69], v[68:69], v[76:77]\n"
    "v_pk_add_f32 v[70:71], v[70:71], v[78:79]\n"
    "v_cvt_pk_bf16_f32 v98, v68, v69\n"
    "v_cvt_pk_bf16_f32 v99, v70, v71\n"
    "v_mfma_f32_16x16x16_bf16 v[104:107], v[96:97], v[44:45], 0\n"
    "s_nop 0\n"
    "v_mfma_f32_16x16x16_bf16 v[112:115], v[98:99], v[48:49], 0\n"
    "v_mfma_f32_16x16x16_bf16 v[108:111], v[96:97], v[46:47], 0\n"
    "v_mfma_f32_16x16x16_bf16 v[116:119], v[98:99], v[50:51], 0\n"
    "s_lshl_b32 s74, s57, 14\n"
    "s_add_u32 s74, s74, 0xd00000\n"
    "s_lshl_b32 s75, s58, 2\n"
    "s_add_u32 s75, s75, s59\n"
    "s_lshl_b32 s75, s75, 8\n"
    "s_add_u32 s12, s74, s75\n"
    "v_mov_b32 v60, s62\n"
    "v_lshlrev_b32 v253, 2, v23\n"
    "v_add_f32 v120, 0x358637bd, v120\n"
    "v_add_f32 v128, 0x358637bd, v128\n"
    "v_add_f32 v124, 0x358637bd, v124\n"
    "v_add_f32 v132, 0x358637bd, v132\n"
    "v_rcp_f32 v120, v120\n"
    "v_rcp_f32 v128, v128\n"
    "v_rcp_f32 v124, v124\n"
    "v_rcp_f32 v132, v132\n"
    "v_pk_mul_f32 v[104:105], v[104:105], v[120:121] op_sel_hi:[1,0]\n"
    "v_pk_mul_f32 v[106:107], v[106:107], v[120:121] op_sel_hi:[1,0]\n"
    "v_pk_mul_f32 v[112:113], v[112:113], v[128:129] op_sel_hi:[1,0]\n"
    "v_pk_mul_f32 v[114:115], v[114:115], v[128:129] op_sel_hi:[1,0]\n"
    "v_cvt_pk_bf16_f32 v136, v104, v105\n"
    "v_cvt_pk_bf16_f32 v137, v106, v107\n"
    "v_cvt_pk_bf16_f32 v138, v112, v113\n"
    "v_cvt_pk_bf16_f32 v139, v114, v115\n"
    "buffer_store_dwordx4 v[136:139], v247, s[52:55], s89 offen sc1\n"
    "v_pk_mul_f32 v[108:109], v[108:109], v[124:125] op_sel_hi:[1,0]\n"
    "v_pk_mul_f32 v[110:111], v[110:111], v[124:125] op_sel_hi:[1,0]\n"
    "v_pk_mul_f32 v[116:117], v[116:117], v[132:133] op_sel_hi:[1,0]\n"
    "v_pk_mul_f32 v[118:119], v[118:119], v[132:133] op_sel_hi:[1,0]\n"
    "v_cvt_pk_bf16_f32 v140, v108, v109\n"
    "v_cvt_pk_bf16_f32 v141, v110, v111\n"
    "v_cvt_pk_bf16_f32 v142, v116, v117\n"
    "v_cvt_pk_bf16_f32 v143, v118, v119\n"
    "s_add_u32 s65, s89, 0x1000\n"
    "buffer_store_dwordx4 v[140:143], v247, s[52:55], s65 offen sc1\n"
    "v_lshrrev_b32 v61, 1, v21\n"
    "v_lshrrev_b32 v62, 1, v22\n"
    "v_xor_b32 v61, v61, v62\n"
    "v_lshlrev_b32 v61, 4, v61\n"
    "v_lshl_add_u32 v61, v21, 7, v61\n"
    "v_and_b32 v62, 1, v22\n"
    "v_lshl_add_u32 v61, v62, 3, v61\n"
    "v_add_u32 v61, s86, v61\n"
    "ds_read_b64 v[52:53], v61\n"
    "v_xor_b32 v62, 32, v61\n"
    "ds_read_b64 v[54:55], v62\n"
    "v_xor_b32 v62, 64, v61\n"
    "ds_read_b64 v[56:57], v62\n"
    "v_xor_b32 v62, 0x60, v61\n"
    "ds_read_b64 v[58:59], v62\n"
    "s_lshr_b32 s75, s59, 1\n"
    "s_lshl_b32 s76, s60, 1\n"
    "s_add_u32 s75, s75, s76\n"
    "s_lshl_b32 s75, s75, 8\n"
    "v_lshl_add_u32 v254, v21, 10, s75\n"
    "ds_read_b64_tr_b16 a[96:97], v6 offset:8192\n"
    "ds_read_b64_tr_b16 a[98:99], v6 offset:10240\n"
    "s_add_u32 m0, s82, 0\n"
    "s_nop 0\n"
    "buffer_load_dwordx4 v14, s[32:35], s81 offen lds\n"
    "ds_read_b64_tr_b16 a[100:101], v7 offset:8192\n"
    "ds_read_b64_tr_b16 a[102:103], v7 offset:10240\n"
    "ds_read_b64_tr_b16 a[104:105], v8 offset:8192\n"
    "ds_read_b64_tr_b16 a[106:107], v8 offset:10240\n"
    "ds_read_b64_tr_b16 a[108:109], v9 offset:8192\n"
    "ds_read_b64_tr_b16 a[110:111], v9 offset:10240\n"
    "ds_read_b64_tr_b16 a[112:113], v6 offset:12288\n"
    "ds_read_b64_tr_b16 a[114:115], v6 offset:14336\n"
    "s_addk_i32 m0, 4096\n"
    "s_add_u32 s65, s81, 0x8000\n"
    "buffer_load_dwordx4 v14, s[32:35], s65 offen lds\n"
    "ds_read_b64_tr_b16 a[116:117], v7 offset:12288\n"
    "ds_read_b64_tr_b16 a[118:119], v7 offset:14336\n"
    "ds_read_b64_tr_b16 a[120:121], v8 offset:12288\n"
    "ds_read_b64_tr_b16 a[122:123], v8 offset:14336\n"
    "ds_read_b64_tr_b16 a[124:125], v9 offset:12288\n"
    "ds_read_b64_tr_b16 a[126:127], v9 offset:14336\n"
    "ds_read_b64_tr_b16 a[128:129], v6 offset:16384\n"
    "ds_read_b64_tr_b16 a[130:131], v6 offset:18432\n"
    "ds_read_b64_tr_b16 a[132:133], v7 offset:16384\n"
    "ds_read_b64_tr_b16 a[134:135], v7 offset:18432\n"
    "s_addk_i32 m0, 20480\n"
    "s_add_u32 s65, s81, 0x30000\n"
    "buffer_load_dwordx4 v14, s[32:35], s65 offen lds\n"
    "ds_read_b64_tr_b16 a[136:137], v8 offset:16384\n"
    "ds_read_b64_tr_b16 a[138:139], v8 offset:18432\n"
    "ds_read_b64_tr_b16 a[140:141], v9 offset:16384\n"
    "ds_read_b64_tr_b16 a[142:143], v9 offset:18432\n"
    "ds_read_b64_tr_b16 a[144:145], v6 offset:20480\n"
    "ds_read_b64_tr_b16 a[146:147], v6 offset:22528\n"
    "ds_read_b64_tr_b16 a[148:149], v7 offset:20480\n"
    "ds_read_b64_tr_b16 a[150:151], v7 offset:22528\n"
    "ds_read_b64_tr_b16 a[152:153], v8 offset:20480\n"
    "ds_read_b64_tr_b16 a[154:155], v8 offset:22528\n"
    "s_addk_i32 m0, 4096\n"
    "s_add_u32 s65, s81, 0x38000\n"
    "buffer_load_dwordx4 v14, s[32:35], s65 offen lds\n"
    "ds_read_b64_tr_b16 a[156:157], v9 offset:20480\n"
    "ds_read_b64_tr_b16 a[158:159], v9 offset:22528\n"
    "ds_read_b64_tr_b16 a[192:193], v6 offset:32768\n"
    "ds_read_b64_tr_b16 a[194:195], v6 offset:34816\n"
    "ds_read_b64_tr_b16 a[196:197], v7 offset:32768\n"
    "ds_read_b64_tr_b16 a[198:199], v7 offset:34816\n"
    "ds_read_b64_tr_b16 a[200:201], v8 offset:32768\n"
    "ds_read_b64_tr_b16 a[202:203], v8 offset:34816\n"
    "s_addk_i32 m0, 20480\n"
    "s_add_u32 s65, s81, 0x60000\n"
    "buffer_load_dwordx4 v14, s[32:35], s65 offen lds\n"
    "ds_read_b64_tr_b16 a[204:205], v9 offset:32768\n"
    "ds_read_b64_tr_b16 a[206:207], v9 offset:34816\n"
    "ds_read_b64_tr_b16 a[208:209], v6 offset:36864\n"
    "ds_read_b64_tr_b16 a[210:211], v6 offset:38912\n"
    "ds_read_b64_tr_b16 a[212:213], v7 offset:36864\n"
    "ds_read_b64_tr_b16 a[214:215], v7 offset:38912\n"
    "ds_read_b64_tr_b16 a[216:217], v8 offset:36864\n"
    "ds_read_b64_tr_b16 a[218:219], v8 offset:38912\n"
    "ds_read_b64_tr_b16 a[220:221], v9 offset:36864\n"
    "ds_read_b64_tr_b16 a[222:223], v9 offset:38912\n"
    "s_addk_i32 m0, 4096\n"
    "s_add_u32 s65, s81, 0x68000\n"
    "buffer_load_dwordx4 v14, s[32:35], s65 offen lds\n"
    "ds_read_b64_tr_b16 a[224:225], v6 offset:40960\n"
    "ds_read_b64_tr_b16 a[226:227], v6 offset:43008\n"
    "ds_read_b64_tr_b16 a[228:229], v7 offset:40960\n"
    "ds_read_b64_tr_b16 a[230:231], v7 offset:43008\n"
    "ds_read_b64_tr_b16 a[232:233], v8 offset:40960\n"
    "ds_read_b64_tr_b16 a[234:235], v8 offset:43008\n"
    "ds_read_b64_tr_b16 a[236:237], v9 offset:40960\n"
    "ds_read_b64_tr_b16 a[238:239], v9 offset:43008\n"
    "s_waitcnt vmcnt(6)\n"
    "s_mov_b32 exec_lo, 0xffffffff\n"
    "s_mov_b32 exec_hi, 0x0\n"
    "buffer_store_dword v60, v253, s[52:55], s12 offen sc1\n"
    "s_mov_b64 exec, -1\n"
    "s_waitcnt lgkmcnt(15)\n"
    "s_barrier\n"
    "ds_read_b64_tr_b16 a[240:241], v6 offset:45056\n"
    "ds_read_b64_tr_b16 a[242:243], v6 offset:47104\n"
    "s_add_u32 m0, s84, 0xf000\n"
    "s_add_u32 s66, s83, 0xd7000\n"
    "buffer_load_dwordx4 v20, s[36:39], s66 offen lds\n"
    "ds_read_b64_tr_b16 a[244:245], v7 offset:45056\n"
    "ds_read_b64_tr_b16 a[246:247], v7 offset:47104\n"
    "s_addk_i32 s66, 9984\n"
    "buffer_load_dwordx4 v233, s[36:39], s66 offen offset:1024 lds\n"
    "ds_read_b64_tr_b16 a[248:249], v8 offset:45056\n"
    "ds_read_b64_tr_b16 a[250:251], v8 offset:47104\n"
    "s_addk_i32 s66, -12032\n"
    "buffer_load_dwordx4 v234, s[36:39], s66 offen offset:2048 lds\n"
    "ds_read_b64_tr_b16 a[252:253], v9 offset:45056\n"
    "ds_read_b64_tr_b16 a[254:255], v9 offset:47104\n"
    "s_addk_i32 m0, 12288\n"
    "s_add_u32 s66, s83, 0x102000\n"
    "buffer_load_dwordx4 v20, s[36:39], s66 offen lds\n"
    "ds_read_b64_tr_b16 v[96:97], v6 offset:57344\n"
    "ds_read_b64_tr_b16 v[98:99], v6 offset:59392\n"
    "ds_read_b64_tr_b16 v[100:101], v7 offset:57344\n"
    "ds_read_b64_tr_b16 v[102:103], v7 offset:59392\n"
    "s_addk_i32 s66, 9984\n"
    "buffer_load_dwordx4 v233, s[36:39], s66 offen offset:1024 lds\n"
    "ds_read_b64_tr_b16 v[104:105], v8 offset:57344\n"
    "ds_read_b64_tr_b16 v[106:107], v8 offset:59392\n"
    "s_addk_i32 s66, -12032\n"
    "buffer_load_dwordx4 v234, s[36:39], s66 offen offset:2048 lds\n"
    "ds_read_b64_tr_b16 v[108:109], v9 offset:57344\n"
    "ds_read_b64_tr_b16 v[110:111], v9 offset:59392\n"
    "ds_read_b64_tr_b16 v[112:113], v6 offset:61440\n"
    "ds_read_b64_tr_b16 v[114:115], v6 offset:63488\n"
    "s_addk_i32 m0, 12288\n"
    "s_add_u32 s66, s83, 0x12d000\n"
    "buffer_load_dwordx4 v20, s[36:39], s66 offen lds\n"
    "ds_read_b64_tr_b16 v[116:117], v7 offset:61440\n"
    "ds_read_b64_tr_b16 v[118:119], v7 offset:63488\n"
    "ds_read_b64_tr_b16 v[120:121], v8 offset:61440\n"
    "ds_read_b64_tr_b16 v[122:123], v8 offset:63488\n"
    "s_addk_i32 s66, 9984\n"
    "buffer_load_dwordx4 v233, s[36:39], s66 offen offset:1024 lds\n"
    "ds_read_b64_tr_b16 v[124:125], v9 offset:61440\n"
    "ds_read_b64_tr_b16 v[126:127], v9 offset:63488\n"
    "s_addk_i32 s66, -12032\n"
    "buffer_load_dwordx4 v234, s[36:39], s66 offen offset:2048 lds\n"
    "s_waitcnt vmcnt(10)\n"
    "s_barrier\n"
    "ds_read_b64_tr_b16 a[64:65], v6\n"
    "ds_read_b64_tr_b16 a[66:67], v6 offset:2048\n"
    "s_add_u32 m0, s84, 0\n"
    "s_nop 0\n"
    "buffer_load_dwordx4 v20, s[36:39], s83 offen lds\n"
    "ds_read_b64_tr_b16 a[68:69], v7\n"
    "ds_read_b64_tr_b16 a[70:71], v7 offset:2048\n"
    "s_add_u32 s66, s83, 0x2700\n"
    "buffer_load_dwordx4 v233, s[36:39], s66 offen offset:1024 lds\n"
    "ds_read_b64_tr_b16 a[72:73], v8\n"
    "ds_read_b64_tr_b16 a[74:75], v8 offset:2048\n"
    "ds_read_b64_tr_b16 a[76:77], v9\n"
    "ds_read_b64_tr_b16 a[78:79], v9 offset:2048\n"
    "s_addk_i32 m0, 2048\n"
    "s_nop 0\n"
    "buffer_load_dwordx4 v234, s[36:39], s83 offen lds\n"
    "ds_read_b64_tr_b16 a[80:81], v6 offset:4096\n"
    "ds_read_b64_tr_b16 a[82:83], v6 offset:6144\n"
    "s_addk_i32 m0, 10240\n"
    "s_add_u32 s66, s83, 0x2b000\n"
    "buffer_load_dwordx4 v20, s[36:39], s66 offen lds\n"
    "ds_read_b64_tr_b16 a[84:85], v7 offset:4096\n"
    "ds_read_b64_tr_b16 a[86:87], v7 offset:6144\n"
    "s_addk_i32 s66, 9984\n"
    "buffer_load_dwordx4 v233, s[36:39], s66 offen offset:1024 lds\n"
    "ds_read_b64_tr_b16 a[88:89], v8 offset:4096\n"
    "ds_read_b64_tr_b16 a[90:91], v8 offset:6144\n"
    "s_addk_i32 s66, -12032\n"
    "buffer_load_dwordx4 v234, s[36:39], s66 offen offset:2048 lds\n"
    "ds_read_b64_tr_b16 a[92:93], v9 offset:4096\n"
    "ds_read_b64_tr_b16 a[94:95], v9 offset:6144\n"
    "ds_read_b64_tr_b16 a[160:161], v6 offset:24576\n"
    "ds_read_b64_tr_b16 a[162:163], v6 offset:26624\n"
    "ds_read_b64_tr_b16 a[164:165], v7 offset:24576\n"
    "ds_read_b64_tr_b16 a[166:167], v7 offset:26624\n"
    "ds_read_b64_tr_b16 a[168:169], v8 offset:24576\n"
    "ds_read_b64_tr_b16 a[170:171], v8 offset:26624\n"
    "ds_read_b64_tr_b16 a[172:173], v9 offset:24576\n"
    "ds_read_b64_tr_b16 a[174:175], v9 offset:26624\n"
    "s_and_b32 s75, s87, 0x7f\n"
    "s_lshl_b32 s75, s75, 8\n"
    "s_add_u32 s19, s18, s75\n"
    "s_add_u32 s19, s19, 0x1000000\n"
    "s_lshl_b32 s75, s64, 6\n"
    "v_lshlrev_b32 v61, 2, v23\n"
    "buffer_load_dword v152, v61, s[40:43], s75 offen\n"
    "s_mov_b64 exec, 0xffff\n"
    "buffer_load_dword v255, v254, s[52:55], s74 offen sc1\n"
    "s_mov_b64 exec, -1\n"
    "ds_read_b64_tr_b16 a[176:177], v6 offset:28672\n"
    "ds_read_b64_tr_b16 a[178:179], v6 offset:30720\n"
    "ds_read_b64_tr_b16 a[180:181], v7 offset:28672\n"
    "ds_read_b64_tr_b16 a[182:183], v7 offset:30720\n"
    "ds_read_b64_tr_b16 a[184:185], v8 offset:28672\n"
    "ds_read_b64_tr_b16 a[186:187], v8 offset:30720\n"
    "ds_read_b64_tr_b16 a[188:189], v9 offset:28672\n"
    "ds_read_b64_tr_b16 a[190:191], v9 offset:30720\n"
    "ds_read_b64_tr_b16 v[64:65], v6 offset:49152\n"
    "ds_read_b64_tr_b16 v[66:67], v6 offset:51200\n"
    "ds_read_b64_tr_b16 v[68:69], v7 offset:49152\n"
    "ds_read_b64_tr_b16 v[70:71], v7 offset:51200\n"
    "ds_read_b64_tr_b16 v[72:73], v8 offset:49152\n"
    "ds_read_b64_tr_b16 v[74:75], v8 offset:51200\n"
    "ds_read_b64_tr_b16 v[76:77], v9 offset:49152\n"
    "ds_read_b64_tr_b16 v[78:79], v9 offset:51200\n"
    "ds_read_b64_tr_b16 v[80:81], v6 offset:53248\n"
    "ds_read_b64_tr_b16 v[82:83], v6 offset:55296\n"
    "ds_read_b64_tr_b16 v[84:85], v7 offset:53248\n"
    "ds_read_b64_tr_b16 v[86:87], v7 offset:55296\n"
    "ds_read_b64_tr_b16 v[88:89], v8 offset:53248\n"
    "ds_read_b64_tr_b16 v[90:91], v8 offset:55296\n"
    "ds_read_b64_tr_b16 v[92:93], v9 offset:53248\n"
    "ds_read_b64_tr_b16 v[94:95], v9 offset:55296\n"
    "s_mov_b32 s69, 0\n"
    "s_waitcnt vmcnt(0)\n"
    "v_cmp_ne_u32 vcc, s62, v255\n"
    "s_nop 0\n"
    "s_and_b32 vcc_lo, vcc_lo, 0xffff\n"
    "s_cbranch_scc0 .Ld10_%=\n"
    ".Lp8_%=:\n"
    "s_mov_b64 exec, 0xffff\n"
    "buffer_load_dword v255, v254, s[52:55], s74 offen sc1\n"
    "s_mov_b64 exec, -1\n"
    "s_waitcnt vmcnt(0)\n"
    "v_cmp_ne_u32 vcc, s62, v255\n"
    "s_nop 0\n"
    "s_and_b32 vcc_lo, vcc_lo, 0xffff\n"
    "s_cbranch_scc0 .Ld10_%=\n"
    "s_add_u32 s69, s69, 1\n"
    "s_cmp_lt_u32 s69, 0x20000\n"
    "s_cbranch_scc1 .Lp8_%=\n"
    "s_nop 7\n"
    ".Ld10_%=:\n"
    "buffer_load_dwordx4 a[0:3], v247, s[52:55], s19 offen sc1\n"
    "buffer_load_dwordx4 a[4:7], v247, s[52:55], s19 offen offset:256 sc1\n"
    "buffer_load_dwordx4 a[8:11], v247, s[52:55], s19 offen offset:512 sc1\n"
    "buffer_load_dwordx4 a[12:15], v247, s[52:55], s19 offen offset:768 sc1\n"
    "buffer_load_dwordx4 a[16:19], v247, s[52:55], s19 offen offset:1024 sc1\n"
    "buffer_load_dwordx4 a[20:23], v247, s[52:55], s19 offen offset:1280 sc1\n"
    "buffer_load_dwordx4 a[24:27], v247, s[52:55], s19 offen offset:1536 sc1\n"
    "buffer_load_dwordx4 a[28:31], v247, s[52:55], s19 offen offset:1792 sc1\n"
    "buffer_load_dwordx4 a[32:35], v247, s[52:55], s19 offen offset:2048 sc1\n"
    "v_lshlrev_b32 v28, 16, v52\n"
    "v_and_b32 v29, 0xffff0000, v52\n"
    "v_lshlrev_b32 v30, 16, v53\n"
    "v_and_b32 v31, 0xffff0000, v53\n"
    "v_lshlrev_b32 v32, 16, v54\n"
    "v_and_b32 v33, 0xffff0000, v54\n"
    "v_lshlrev_b32 v34, 16, v55\n"
    "v_and_b32 v35, 0xffff0000, v55\n"
    "v_lshlrev_b32 v36, 16, v56\n"
    "v_and_b32 v37, 0xffff0000, v56\n"
    "v_lshlrev_b32 v38, 16, v57\n"
    "v_and_b32 v39, 0xffff0000, v57\n"
    "v_lshlrev_b32 v40, 16, v58\n"
    "v_and_b32 v41, 0xffff0000, v58\n"
    "v_lshlrev_b32 v42, 16, v59\n"
    "v_and_b32 v43, 0xffff0000, v59\n"
    "s_mul_i32 s81, s59, 0x58000\n"
    "s_lshl_b32 s75, s61, 7\n"
    "s_add_u32 s81, s81, s75\n"
    "s_lshl_b32 s82, s59, 14\n"
    "s_add_u32 s82, s82, 0x18000\n"
    "v_lshrrev_b32 v148, 5, v23\n"
    "v_and_b32 v149, 1, v22\n"
    "v_bfe_u32 v150, v23, 2, 2\n"
    "v_and_b32 v151, 3, v23\n"
    "v_bfe_u32 v61, v23, 4, 1\n"
    "v_lshlrev_b32 v61, 2, v61\n"
    "v_and_b32 v62, 7, v23\n"
    "v_xor_b32 v61, v61, v62\n"
    "v_lshlrev_b32 v61, 4, v61\n"
    "v_lshrrev_b32 v62, 3, v23\n"
    "v_lshl_add_u32 v248, v62, 10, v61\n"
    "v_xor_b32 v63, v149, v148\n"
    "v_lshlrev_b32 v63, 5, v63\n"
    "v_lshl_add_u32 v63, v151, 3, v63\n"
    "v_lshl_add_u32 v61, v148, 10, v63\n"
    "v_lshl_add_u32 v61, v150, 7, v61\n"
    "v_lshrrev_b32 v62, 1, v150\n"
    "v_lshl_add_u32 v250, v62, 6, v61\n"
    "v_lshl_add_u32 v61, v148, 8, v63\n"
    "v_lshl_add_u32 v61, v62, 6, v61\n"
    "v_and_b32 v62, 1, v150\n"
    "v_lshl_add_u32 v61, v62, 7, v61\n"
    "v_add_u32 v61, 0x800, v61\n"
    "v_add_u32 v62, s64, v150\n"
    "v_and_b32 v62, 1, v62\n"
    "v_cmp_ne_u32 vcc, 0, v62\n"
    "v_xor_b32 v62, 64, v250\n"
    "s_nop 0\n"
    "v_cndmask_b32 v249, v62, v61, vcc\n"
    "v_cndmask_b32 v251, v61, v62, vcc\n"
    "v_add_u32 v244, 0xc000, v249\n"
    "v_add_u32 v245, 0xc000, v250\n"
    "v_add_u32 v246, 0xc000, v251\n"
    "v_lshlrev_b32 v63, 9, v148\n"
    "v_lshl_add_u32 v63, v149, 5, v63\n"
    "v_lshl_add_u32 v63, v151, 3, v63\n"
    "v_lshl_add_u32 v61, v149, 10, v63\n"
    "v_lshl_add_u32 v61, v150, 7, v61\n"
    "v_lshrrev_b32 v62, 1, v150\n"
    "v_lshl_add_u32 v157, v62, 6, v61\n"
    "v_lshl_add_u32 v61, v149, 8, v63\n"
    "v_lshl_add_u32 v61, v62, 6, v61\n"
    "v_and_b32 v62, 1, v150\n"
    "v_lshl_add_u32 v61, v62, 7, v61\n"
    "v_add_u32 v61, 0x800, v61\n"
    "v_add_u32 v62, s64, v150\n"
    "v_and_b32 v62, 1, v62\n"
    "v_cmp_ne_u32 vcc, 0, v62\n"
    "v_xor_b32 v62, 64, v157\n"
    "s_nop 0\n"
    "v_cndmask_b32 v156, v62, v61, vcc\n"
    "v_cndmask_b32 v158, v61, v62, vcc\n"
    "v_and_b32 v61, 2, v150\n"
    "v_xor_b32 v61, v61, v149\n"
    "v_lshlrev_b32 v61, 5, v61\n"
    "v_lshl_add_u32 v62, v148, 2, v150\n"
    "v_lshl_add_u32 v61, v62, 7, v61\n"
    "v_lshl_add_u32 v61, v151, 3, v61\n"
    "s_mul_i32 s90, s59, 0x5000\n"
    "v_add_u32 v10, s82, v61\n"
    "v_xor_b32 v11, 64, v10\n"
    "v_add_u32 v12, s90, v61\n"
    "v_xor_b32 v13, 64, v12\n"
    "s_and_b32 s75, s59, 1\n"
    "s_lshl_b32 s75, s75, 5\n"
    "s_cmp_gt_u32 s59, 1\n"
    "s_cselect_b64 vcc, -1, 0\n"
    "s_cselect_b32 s76, 0xc000, 0\n"
    "v_cndmask_b32 v144, v156, v157, vcc\n"
    "v_xor_b32 v144, s75, v144\n"
    "v_add_u32 v145, 0xc000, v144\n"
    "v_xor_b32 v146, s75, v158\n"
    "v_add_u32 v146, s76, v146\n"
    "s_bfe_u32 s75, s87, 0x20005\n"
    "s_lshl_b32 s75, s75, 3\n"
    "s_add_u32 s75, s75, s61\n"
    "s_lshl_b32 s75, s75, 10\n"
    "s_add_u32 s89, s18, s75\n"
    "s_and_b32 s75, s59, 1\n"
    "s_lshl_b32 s75, s75, 16\n"
    "s_add_u32 s89, s89, s75\n"
    "s_add_u32 s89, s89, 0x1400000\n"
    "v_and_b32 v61, 0x8070, v247\n"
    "v_lshl_add_u32 v61, v149, 17, v61\n"
    "v_lshl_add_u32 v159, v148, 8, v61\n"
    "s_lshl_b32 s75, s59, 6\n"
    "s_add_u32 s75, s75, 0x27000\n"
    "v_lshl_add_u32 v147, v22, 4, s75\n"
    "s_waitcnt vmcnt(8)\n"
    "s_waitcnt lgkmcnt(15)\n"
    "v_mfma_f32_16x16x32_bf16 v[28:31], a[64:67], a[0:3], v[28:31]\n"
    "v_mfma_f32_16x16x32_bf16 v[32:35], a[68:71], a[0:3], v[32:35]\n"
    "buffer_load_dwordx4 a[36:39], v247, s[52:55], s19 offen offset:2304 sc1\n"
    "v_mfma_f32_16x16x32_bf16 v[36:39], a[72:75], a[0:3], v[36:39]\n"
    "v_mfma_f32_16x16x32_bf16 v[40:43], a[76:79], a[0:3], v[40:43]\n"
    "s_waitcnt vmcnt(8)\n"
    "v_mfma_f32_16x16x32_bf16 v[28:31], a[80:83], a[4:7], v[28:31]\n"
    "v_mfma_f32_16x16x32_bf16 v[32:35], a[84:87], a[4:7], v[32:35]\n"
    "buffer_load_dwordx4 a[40:43], v247, s[52:55], s19 offen offset:2560 sc1\n"
    "v_mfma_f32_16x16x32_bf16 v[36:39], a[88:91], a[4:7], v[36:39]\n"
    "v_mfma_f32_16x16x32_bf16 v[40:43], a[92:95], a[4:7], v[40:43]\n"
    "s_waitcnt vmcnt(8)\n"
    "v_mfma_f32_16x16x32_bf16 v[28:31], a[96:99], a[8:11], v[28:31]\n"
    "v_mfma_f32_16x16x32_bf16 v[32:35], a[100:103], a[8:11], v[32:35]\n"
    "buffer_load_dwordx4 a[44:47], v247, s[52:55], s19 offen offset:2816 sc1\n"
    "v_mfma_f32_16x16x32_bf16 v[36:39], a[104:107], a[8:11], v[36:39]\n"
    "v_mfma_f32_16x16x32_bf16 v[40:43], a[108:111], a[8:11], v[40:43]\n"
    "s_waitcnt vmcnt(8)\n"
    "v_mfma_f32_16x16x32_bf16 v[28:31], a[112:115], a[12:15], v[28:31]\n"
    "v_mfma_f32_16x16x32_bf16 v[32:35], a[116:119], a[12:15], v[32:35]\n"
    "buffer_load_dwordx4 a[48:51], v247, s[52:55], s19 offen offset:3072 sc1\n"
    "v_mfma_f32_16x16x32_bf16 v[36:39], a[120:123], a[12:15], v[36:39]\n"
    "v_mfma_f32_16x16x32_bf16 v[40:43], a[124:127], a[12:15], v[40:43]\n"
    "s_waitcnt vmcnt(8)\n"
    "v_mfma_f32_16x16x32_bf16 v[28:31], a[128:131], a[16:19], v[28:31]\n"
    "v_mfma_f32_16x16x32_bf16 v[32:35], a[132:135], a[16:19], v[32:35]\n"
    "buffer_load_dwordx4 a[52:55], v247, s[52:55], s19 offen offset:3328 sc1\n"
    "v_mfma_f32_16x16x32_bf16 v[36:39], a[136:139], a[16:19], v[36:39]\n"
    "v_mfma_f32_16x16x32_bf16 v[40:43], a[140:143], a[16:19], v[40:43]\n"
    "s_waitcnt vmcnt(8)\n"
    "v_mfma_f32_16x16x32_bf16 v[28:31], a[144:147], a[20:23], v[28:31]\n"
    "v_mfma_f32_16x16x32_bf16 v[32:35], a[148:151], a[20:23], v[32:35]\n"
    "buffer_load_dwordx4 a[56:59], v247, s[52:55], s19 offen offset:3584 sc1\n"
    "v_mfma_f32_16x16x32_bf16 v[36:39], a[152:155], a[20:23], v[36:39]\n"
    "v_mfma_f32_16x16x32_bf16 v[40:43], a[156:159], a[20:23], v[40:43]\n"
    "s_waitcnt vmcnt(8)\n"
    "v_mfma_f32_16x16x32_bf16 v[28:31], a[160:163], a[24:27], v[28:31]\n"
    "v_mfma_f32_16x16x32_bf16 v[32:35], a[164:167], a[24:27], v[32:35]\n"
    "buffer_load_dwordx4 a[60:63], v247, s[52:55], s19 offen offset:3840 sc1\n"
    "v_mfma_f32_16x16x32_bf16 v[36:39], a[168:171], a[24:27], v[36:39]\n"
    "v_mfma_f32_16x16x32_bf16 v[40:43], a[172:175], a[24:27], v[40:43]\n"
    "s_waitcnt vmcnt(8)\n"
    "v_mfma_f32_16x16x32_bf16 v[28:31], a[176:179], a[28:31], v[28:31]\n"
    "v_mfma_f32_16x16x32_bf16 v[32:35], a[180:183], a[28:31], v[32:35]\n"
    "v_mfma_f32_16x16x32_bf16 v[36:39], a[184:187], a[28:31], v[36:39]\n"
    "v_mfma_f32_16x16x32_bf16 v[40:43], a[188:191], a[28:31], v[40:43]\n"
    "s_waitcnt vmcnt(7)\n"
    "v_mfma_f32_16x16x32_bf16 v[28:31], a[192:195], a[32:35], v[28:31]\n"
    "v_mfma_f32_16x16x32_bf16 v[32:35], a[196:199], a[32:35], v[32:35]\n"
    "v_mfma_f32_16x16x32_bf16 v[36:39], a[200:203], a[32:35], v[36:39]\n"
    "v_mfma_f32_16x16x32_bf16 v[40:43], a[204:207], a[32:35], v[40:43]\n"
    "s_waitcnt vmcnt(6)\n"
    "v_mfma_f32_16x16x32_bf16 v[28:31], a[208:211], a[36:39], v[28:31]\n"
    "v_mfma_f32_16x16x32_bf16 v[32:35], a[212:215], a[36:39], v[32:35]\n"
    "v_mfma_f32_16x16x32_bf16 v[36:39], a[216:219], a[36:39], v[36:39]\n"
    "v_mfma_f32_16x16x32_bf16 v[40:43], a[220:223], a[36:39], v[40:43]\n"
    "s_waitcnt vmcnt(5)\n"
    "v_mfma_f32_16x16x32_bf16 v[28:31], a[224:227], a[40:43], v[28:31]\n"
    "v_mfma_f32_16x16x32_bf16 v[32:35], a[228:231], a[40:43], v[32:35]\n"
    "v_mfma_f32_16x16x32_bf16 v[36:39], a[232:235], a[40:43], v[36:39]\n"
    "v_mfma_f32_16x16x32_bf16 v[40:43], a[236:239], a[40:43], v[40:43]\n"
    "s_waitcnt vmcnt(4)\n"
    "v_mfma_f32_16x16x32_bf16 v[28:31], a[240:243], a[44:47], v[28:31]\n"
    "v_mfma_f32_16x16x32_bf16 v[32:35], a[244:247], a[44:47], v[32:35]\n"
    "v_mfma_f32_16x16x32_bf16 v[36:39], a[248:251], a[44:47], v[36:39]\n"
    "v_mfma_f32_16x16x32_bf16 v[40:43], a[252:255], a[44:47], v[40:43]\n"
    "s_waitcnt vmcnt(3)\n"
    "s_waitcnt lgkmcnt(14)\n"
    "v_mfma_f32_16x16x32_bf16 v[28:31], v[64:67], a[48:51], v[28:31]\n"
    "s_waitcnt lgkmcnt(12)\n"
    "v_mfma_f32_16x16x32_bf16 v[32:35], v[68:71], a[48:51], v[32:35]\n"
    "s_waitcnt lgkmcnt(10)\n"
    "v_mfma_f32_16x16x32_bf16 v[36:39], v[72:75], a[48:51], v[36:39]\n"
    "s_waitcnt lgkmcnt(8)\n"
    "v_mfma_f32_16x16x32_bf16 v[40:43], v[76:79], a[48:51], v[40:43]\n"
    "s_waitcnt vmcnt(2)\n"
    "s_waitcnt lgkmcnt(6)\n"
    "v_mfma_f32_16x16x32_bf16 v[28:31], v[80:83], a[52:55], v[28:31]\n"
    "s_waitcnt lgkmcnt(4)\n"
    "v_mfma_f32_16x16x32_bf16 v[32:35], v[84:87], a[52:55], v[32:35]\n"
    "s_waitcnt lgkmcnt(2)\n"
    "v_mfma_f32_16x16x32_bf16 v[36:39], v[88:91], a[52:55], v[36:39]\n"
    "s_waitcnt lgkmcnt(0)\n"
    "v_mfma_f32_16x16x32_bf16 v[40:43], v[92:95], a[52:55], v[40:43]\n"
    "s_waitcnt vmcnt(1)\n"
    "v_mfma_f32_16x16x32_bf16 v[28:31], v[96:99], a[56:59], v[28:31]\n"
    "v_mfma_f32_16x16x32_bf16 v[32:35], v[100:103], a[56:59], v[32:35]\n"
    "v_mfma_f32_16x16x32_bf16 v[36:39], v[104:107], a[56:59], v[36:39]\n"
    "v_mfma_f32_16x16x32_bf16 v[40:43], v[108:111], a[56:59], v[40:43]\n"
    "s_waitcnt vmcnt(0)\n"
    "v_mfma_f32_16x16x32_bf16 v[28:31], v[112:115], a[60:63], v[28:31]\n"
    "v_mfma_f32_16x16x32_bf16 v[32:35], v[116:119], a[60:63], v[32:35]\n"
    "v_mfma_f32_16x16x32_bf16 v[36:39], v[120:123], a[60:63], v[36:39]\n"
    "v_mfma_f32_16x16x32_bf16 v[40:43], v[124:127], a[60:63], v[40:43]\n"
    "s_nop 4\n"
    "v_cvt_pk_bf16_f32 v128, v28, v29\n"
    "v_cvt_pk_bf16_f32 v129, v30, v31\n"
    "v_cvt_pk_bf16_f32 v130, v32, v33\n"
    "v_cvt_pk_bf16_f32 v131, v34, v35\n"
    "buffer_store_dwordx4 v[128:131], v159, s[52:55], s89 offen sc1\n"
    "v_cvt_pk_bf16_f32 v132, v36, v37\n"
    "v_cvt_pk_bf16_f32 v133, v38, v39\n"
    "v_cvt_pk_bf16_f32 v134, v40, v41\n"
    "v_cvt_pk_bf16_f32 v135, v42, v43\n"
    "buffer_store_dwordx4 v[132:135], v159, s[52:55], s89 offen offset:512 sc1\n"
    "v_pk_add_f32 v[136:137], v[28:29], v[30:31]\n"
    "v_pk_mul_f32 v[138:139], v[28:29], v[28:29]\n"
    "v_pk_fma_f32 v[138:139], v[30:31], v[30:31], v[138:139]\n"
    "v_pk_add_f32 v[136:137], v[136:137], v[32:33]\n"
    "v_pk_fma_f32 v[138:139], v[32:33], v[32:33], v[138:139]\n"
    "v_pk_add_f32 v[136:137], v[136:137], v[34:35]\n"
    "v_pk_fma_f32 v[138:139], v[34:35], v[34:35], v[138:139]\n"
    "v_pk_add_f32 v[136:137], v[136:137], v[36:37]\n"
    "v_pk_fma_f32 v[138:139], v[36:37], v[36:37], v[138:139]\n"
    "v_pk_add_f32 v[136:137], v[136:137], v[38:39]\n"
    "v_pk_fma_f32 v[138:139], v[38:39], v[38:39], v[138:139]\n"
    "v_pk_add_f32 v[136:137], v[136:137], v[40:41]\n"
    "v_pk_fma_f32 v[138:139], v[40:41], v[40:41], v[138:139]\n"
    "v_pk_add_f32 v[136:137], v[136:137], v[42:43]\n"
    "v_pk_fma_f32 v[138:139], v[42:43], v[42:43], v[138:139]\n"
    "v_add_f32 v140, v136, v137\n"
    "v_add_f32 v141, v138, v139\n"
    "v_mov_b32 v142, v140\n"
    "v_mov_b32 v143, v141\n"
    "s_nop 0\n"
    "v_permlane32_swap_b32 v140, v142\n"
    "v_permlane32_swap_b32 v141, v143\n"
    "v_pk_add_f32 v[140:141], v[140:141], v[142:143]\n"
    "v_mov_b32 v142, v140\n"
    "v_mov_b32 v143, v141\n"
    "s_nop 0\n"
    "v_permlane16_swap_b32 v140, v142\n"
    "v_permlane16_swap_b32 v141, v143\n"
    "v_pk_add_f32 v[140:141], v[140:141], v[142:143]\n"
    "s_lshl3_add_u32 s75, s57, s61\n"
    "s_lshl_b32 s75, s75, 10\n"
    "s_and_b32 s76, s87, 0x7f\n"
    "s_lshl_b32 s76, s76, 3\n"
    "s_add_u32 s75, s75, s76\n"
    "v_lshlrev_b32 v61, 3, v21\n"
    "s_mov_b32 exec_lo, 0xffff\n"
    "s_mov_b32 exec_hi, 0x0\n"
    "s_add_u32 s65, s75, 0x500000\n"
    "buffer_store_dwordx2 v[140:141], v61, s[52:55], s65 offen sc1\n"
    "s_mov_b64 exec, -1\n"
    "s_barrier\n"
    "ds_read_b64_tr_b16 v[100:101], v144\n"
    "ds_read_b64_tr_b16 v[102:103], v144 offset:3072\n"
    "ds_read_b64_tr_b16 v[104:105], v144 offset:6144\n"
    "ds_read_b64_tr_b16 v[106:107], v144 offset:9216\n"
    "ds_read_b64_tr_b16 v[108:109], v146\n"
    "ds_read_b64_tr_b16 v[110:111], v146 offset:3072\n"
    "ds_read_b64_tr_b16 v[112:113], v144 offset:12288\n"
    "ds_read_b64_tr_b16 v[114:115], v144 offset:15360\n"
    "ds_read_b64_tr_b16 v[116:117], v144 offset:18432\n"
    "ds_read_b64_tr_b16 v[118:119], v144 offset:21504\n"
    "ds_read_b64_tr_b16 v[120:121], v146 offset:6144\n"
    "ds_read_b64_tr_b16 v[122:123], v146 offset:9216\n"
    "v_lshlrev_b32 v154, 16, v152\n"
    "v_and_b32 v155, 0xffff0000, v152\n"
    "v_mul_f32 v154, s10, v154\n"
    "v_mul_f32 v155, s10, v155\n"
    "v_lshlrev_b32 v61, 3, v23\n"
    "v_add_u32 v61, 0x27000, v61\n"
    "s_lshr_b32 s75, s59, 1\n"
    "s_lshl_b32 s75, s75, 13\n"
    "s_and_b32 s76, s59, 1\n"
    "s_lshl_b32 s76, s76, 9\n"
    "s_add_u32 s75, s75, s76\n"
    "v_and_b32 v254, 7, v23\n"
    "v_lshl_add_u32 v254, v254, 10, s75\n"
    "v_bfe_u32 v255, v23, 3, 1\n"
    "v_lshl_add_u32 v254, v255, 8, v254\n"
    "s_waitcnt lgkmcnt(8)\n"
    "v_mfma_f32_16x16x32_bf16 v[76:79], v[100:103], v[24:27], 0\n"
    "ds_read_b64_tr_b16 v[124:125], v144 offset:24576\n"
    "ds_read_b64_tr_b16 v[126:127], v144 offset:27648\n"
    "v_mfma_f32_16x16x32_bf16 v[76:79], v[104:107], v[24:27], v[76:79]\n"
    "ds_read_b64_tr_b16 v[128:129], v144 offset:30720\n"
    "ds_read_b64_tr_b16 v[130:131], v144 offset:33792\n"
    "s_waitcnt lgkmcnt(8)\n"
    "v_mfma_f32_16x16x32_bf16 v[80:83], v[108:111], v[24:27], 0\n"
    "ds_read_b64_tr_b16 v[100:101], v146 offset:12288\n"
    "ds_read_b64_tr_b16 v[102:103], v146 offset:15360\n"
    "v_mfma_f32_16x16x32_bf16 v[76:79], v[112:115], v[24:27], v[76:79]\n"
    "ds_read_b64_tr_b16 v[104:105], v144 offset:36864\n"
    "ds_read_b64_tr_b16 v[106:107], v144 offset:39936\n"
    "s_waitcnt lgkmcnt(8)\n"
    "v_mfma_f32_16x16x32_bf16 v[76:79], v[116:119], v[24:27], v[76:79]\n"
    "ds_read_b64_tr_b16 v[108:109], v144 offset:43008\n"
    "ds_read_b64_tr_b16 v[110:111], v144 offset:46080\n"
    "v_mfma_f32_16x16x32_bf16 v[80:83], v[120:123], v[24:27], v[80:83]\n"
    "ds_read_b64_tr_b16 v[112:113], v146 offset:18432\n"
    "ds_read_b64_tr_b16 v[114:115], v146 offset:21504\n"
    "s_waitcnt lgkmcnt(8)\n"
    "v_mfma_f32_16x16x32_bf16 v[76:79], v[124:127], v[24:27], v[76:79]\n"
    "ds_read_b64_tr_b16 v[116:117], v144 offset:49152\n"
    "ds_read_b64_tr_b16 v[118:119], v144 offset:52224\n"
    "v_mfma_f32_16x16x32_bf16 v[76:79], v[128:131], v[24:27], v[76:79]\n"
    "ds_read_b64_tr_b16 v[120:121], v144 offset:55296\n"
    "ds_read_b64_tr_b16 v[122:123], v144 offset:58368\n"
    "s_waitcnt lgkmcnt(8)\n"
    "v_mfma_f32_16x16x32_bf16 v[80:83], v[100:103], v[24:27], v[80:83]\n"
    "ds_read_b64_tr_b16 v[124:125], v146 offset:24576\n"
    "ds_read_b64_tr_b16 v[126:127], v146 offset:27648\n"
    "v_mfma_f32_16x16x32_bf16 v[76:79], v[104:107], v[24:27], v[76:79]\n"
    "ds_read_b64_tr_b16 v[128:129], v145 offset:12288\n"
    "ds_read_b64_tr_b16 v[130:131], v145 offset:15360\n"
    "s_waitcnt lgkmcnt(8)\n"
    "v_mfma_f32_16x16x32_bf16 v[76:79], v[108:111], v[24:27], v[76:79]\n"
    "ds_read_b64_tr_b16 v[100:101], v145 offset:18432\n"
    "ds_read_b64_tr_b16 v[102:103], v145 offset:21504\n"
    "v_mfma_f32_16x16x32_bf16 v[80:83], v[112:115], v[24:27], v[80:83]\n"
    "ds_read_b64_tr_b16 v[104:105], v146 offset:30720\n"
    "ds_read_b64_tr_b16 v[106:107], v146 offset:33792\n"
    "s_waitcnt vmcnt(0)\n"
    "s_mov_b32 exec_lo, 0xffffffff\n"
    "s_mov_b32 exec_hi, 0x0\n"
    "s_add_u32 s65, s12, 0x40000\n"
    "buffer_store_dword v60, v253, s[52:55], s65 offen sc1\n"
    "s_mov_b64 exec, -1\n"
    "s_waitcnt lgkmcnt(8)\n"
    "v_mfma_f32_16x16x32_bf16 v[76:79], v[116:119], v[24:27], v[76:79]\n"
    "ds_read_b64_tr_b16 v[108:109], v145 offset:24576\n"
    "ds_read_b64_tr_b16 v[110:111], v145 offset:27648\n"
    "s_add_u32 m0, s82, 0\n"
    "s_nop 0\n"
    "buffer_load_dwordx4 v248, s[44:47], s81 offen lds\n"
    "v_mfma_f32_16x16x32_bf16 v[76:79], v[120:123], v[24:27], v[76:79]\n"
    "ds_read_b64_tr_b16 v[112:113], v145 offset:30720\n"
    "ds_read_b64_tr_b16 v[114:115], v145 offset:33792\n"
    "s_add_u32 s66, s81, 0x1c00\n"
    "buffer_load_dwordx4 v248, s[44:47], s66 offen offset:1024 lds\n"
    "s_waitcnt lgkmcnt(8)\n"
    "v_mfma_f32_16x16x32_bf16 v[80:83], v[124:127], v[24:27], v[80:83]\n"
    "ds_read_b64_tr_b16 v[116:117], v146 offset:36864\n"
    "ds_read_b64_tr_b16 v[118:119], v146 offset:39936\n"
    "v_mfma_f32_16x16x32_bf16 v[76:79], v[128:131], v[24:27], v[76:79]\n"
    "ds_read_b64_tr_b16 v[120:121], v145 offset:36864\n"
    "ds_read_b64_tr_b16 v[122:123], v145 offset:39936\n"
    "s_addk_i32 s66, 7168\n"
    "buffer_load_dwordx4 v248, s[44:47], s66 offen offset:2048 lds\n"
    "s_waitcnt lgkmcnt(8)\n"
    "v_mfma_f32_16x16x32_bf16 v[76:79], v[100:103], v[24:27], v[76:79]\n"
    "ds_read_b64_tr_b16 v[124:125], v145 offset:43008\n"
    "ds_read_b64_tr_b16 v[126:127], v145 offset:46080\n"
    "v_mfma_f32_16x16x32_bf16 v[80:83], v[104:107], v[24:27], v[80:83]\n"
    "ds_read_b64_tr_b16 v[128:129], v146 offset:43008\n"
    "ds_read_b64_tr_b16 v[130:131], v146 offset:46080\n"
    "s_addk_i32 s66, 7168\n"
    "buffer_load_dwordx4 v248, s[44:47], s66 offen offset:3072 lds\n"
    "s_waitcnt lgkmcnt(8)\n"
    "v_mfma_f32_16x16x32_bf16 v[76:79], v[108:111], v[24:27], v[76:79]\n"
    "v_mfma_f32_16x16x32_bf16 v[76:79], v[112:115], v[24:27], v[76:79]\n"
    "s_waitcnt lgkmcnt(4)\n"
    "v_mfma_f32_16x16x32_bf16 v[80:83], v[116:119], v[24:27], v[80:83]\n"
    "s_addk_i32 m0, 4096\n"
    "s_addk_i32 s66, 11264\n"
    "buffer_load_dwordx4 v248, s[44:47], s66 offen lds\n"
    "v_mfma_f32_16x16x32_bf16 v[76:79], v[120:123], v[24:27], v[76:79]\n"
    "s_waitcnt lgkmcnt(0)\n"
    "v_mfma_f32_16x16x32_bf16 v[76:79], v[124:127], v[24:27], v[76:79]\n"
    "s_addk_i32 s66, 7168\n"
    "buffer_load_dwordx4 v248, s[44:47], s66 offen offset:1024 lds\n"
    "v_mfma_f32_16x16x32_bf16 v[80:83], v[128:131], v[24:27], v[80:83]\n"
    "ds_write_b64 v61, v[154:155] offset:512\n"
    "s_nop 3\n"
    "ds_write_b128 v147, v[76:79]\n"
    "s_nop 1\n"
    "ds_write_b128 v147, v[80:83] offset:256\n"
    "s_addk_i32 s66, 7168\n"
    "buffer_load_dwordx4 v248, s[44:47], s66 offen offset:2048 lds\n"
    "s_addk_i32 s66, 7168\n"
    "buffer_load_dwordx4 v248, s[44:47], s66 offen offset:3072 lds\n"
    "s_addk_i32 m0, 4096\n"
    "s_addk_i32 s66, 11264\n"
    "buffer_load_dwordx4 v248, s[44:47], s66 offen lds\n"
    "s_addk_i32 s66, 7168\n"
    "buffer_load_dwordx4 v248, s[44:47], s66 offen offset:1024 lds\n"
    "s_addk_i32 s66, 7168\n"
    "buffer_load_dwordx4 v248, s[44:47], s66 offen offset:2048 lds\n"
    "s_addk_i32 s66, 7168\n"
    "buffer_load_dwordx4 v248, s[44:47], s66 offen offset:3072 lds\n"
    "s_lshl_b32 s75, s59, 13\n"
    "s_add_u32 s83, s18, s75\n"
    "s_lshl_b32 s84, s57, 13\n"
    "s_lshl_b32 s76, s59, 8\n"
    "s_add_u32 s84, s84, s76\n"
    "s_add_u32 s83, s83, 0x1400000\n"
    "s_add_u32 s84, s84, 0x500000\n"
    "v_and_b32 v63, 31, v23\n"
    "v_lshlrev_b32 v63, 3, v63\n"
    "v_lshl_add_u32 v63, v148, 12, v63\n"
    "s_cmp_lg_u32 s56, 0\n"
    "s_cbranch_scc1 .Lo11_%=\n"
    "s_mov_b64 exec, 1\n"
    "buffer_store_dword v60, off, s[52:55], 0 sc1\n"
    "s_mov_b64 exec, -1\n"
    ".Lo11_%=:\n"
    "s_mov_b64 exec, 0xffff\n"
    "s_add_u32 s68, s74, 0x40000\n"
    "buffer_load_dword v255, v254, s[52:55], s68 offen sc1\n"
    "s_mov_b64 exec, -1\n"
    "s_waitcnt lgkmcnt(0)\n"
    "s_barrier\n"
    "v_lshlrev_b32 v62, 4, v148\n"
    "v_add_u32 v62, 0x27000, v62\n"
    "ds_read_b128 v[192:195], v62\n"
    "ds_read_b128 v[196:199], v62 offset:32\n"
    "ds_read_b128 v[200:203], v62 offset:64\n"
    "ds_read_b128 v[204:207], v62 offset:96\n"
    "ds_read_b128 v[208:211], v62 offset:128\n"
    "ds_read_b128 v[212:215], v62 offset:160\n"
    "ds_read_b128 v[216:219], v62 offset:192\n"
    "ds_read_b128 v[220:223], v62 offset:224\n"
    "ds_read_b128 v[224:227], v62 offset:256\n"
    "ds_read_b128 v[228:231], v62 offset:288\n"
    "ds_read_b128 v[232:235], v62 offset:320\n"
    "ds_read_b128 v[236:239], v62 offset:352\n"
    "ds_read_b128 v[76:79], v62 offset:384\n"
    "ds_read_b128 v[80:83], v62 offset:416\n"
    "ds_read_b128 v[84:87], v62 offset:448\n"
    "ds_read_b128 v[88:91], v62 offset:480\n"
    "s_waitcnt lgkmcnt(3)\n"
    "v_pk_add_f32 v[224:225], v[224:225], v[76:77]\n"
    "v_pk_add_f32 v[226:227], v[226:227], v[78:79]\n"
    "s_waitcnt lgkmcnt(2)\n"
    "v_pk_add_f32 v[228:229], v[228:229], v[80:81]\n"
    "v_pk_add_f32 v[230:231], v[230:231], v[82:83]\n"
    "s_waitcnt lgkmcnt(1)\n"
    "v_pk_add_f32 v[232:233], v[232:233], v[84:85]\n"
    "v_pk_add_f32 v[234:235], v[234:235], v[86:87]\n"
    "s_waitcnt lgkmcnt(0)\n"
    "v_pk_add_f32 v[236:237], v[236:237], v[88:89]\n"
    "v_pk_add_f32 v[238:239], v[238:239], v[90:91]\n"
    "ds_read_b64_tr_b16 v[100:101], v249\n"
    "ds_read_b64_tr_b16 v[102:103], v249 offset:512\n"
    "ds_read_b64_tr_b16 v[104:105], v250\n"
    "ds_read_b64_tr_b16 v[106:107], v250 offset:512\n"
    "ds_read_b64_tr_b16 v[108:109], v249 offset:3072\n"
    "ds_read_b64_tr_b16 v[110:111], v249 offset:3584\n"
    "ds_read_b64_tr_b16 v[112:113], v250 offset:3072\n"
    "ds_read_b64_tr_b16 v[114:115], v250 offset:3584\n"
    "ds_read_b64_tr_b16 v[116:117], v249 offset:6144\n"
    "ds_read_b64_tr_b16 v[118:119], v249 offset:6656\n"
    "s_mov_b32 s20, 0x3f596d27\n"
    "s_mov_b32 s21, 0x3f38aa3b\n"
    "s_mov_b32 s22, 0x3f317218\n"
    "s_mov_b32 s69, 0\n"
    "s_waitcnt vmcnt(0)\n"
    "v_cmp_ne_u32 vcc, s62, v255\n"
    "s_nop 0\n"
    "s_and_b32 vcc_lo, vcc_lo, 0xffff\n"
    "s_cbranch_scc0 .Ld14_%=\n"
    ".Lp12_%=:\n"
    "s_mov_b64 exec, 0xffff\n"
    "s_add_u32 s68, s74, 0x40000\n"
    "buffer_load_dword v255, v254, s[52:55], s68 offen sc1\n"
    "s_mov_b64 exec, -1\n"
    "s_waitcnt vmcnt(0)\n"
    "v_cmp_ne_u32 vcc, s62, v255\n"
    "s_nop 0\n"
    "s_and_b32 vcc_lo, vcc_lo, 0xffff\n"
    "s_cbranch_scc0 .Ld14_%=\n"
    "s_add_u32 s69, s69, 1\n"
    "s_cmp_lt_u32 s69, 0x20000\n"
    "s_cbranch_scc1 .Lp12_%=\n"
    "s_nop 7\n"
    ".Ld14_%=:\n"
    "buffer_load_dwordx4 a[128:131], v247, s[52:55], s83 offen sc1\n"
    "buffer_load_dwordx4 a[132:135], v247, s[52:55], s83 offen offset:256 sc1\n"
    "buffer_load_dwordx4 a[136:139], v247, s[52:55], s83 offen offset:512 sc1\n"
    "buffer_load_dwordx4 a[140:143], v247, s[52:55], s83 offen offset:768 sc1\n"
    "buffer_load_dwordx2 v[134:135], v63, s[52:55], s84 offen sc1\n"
    "buffer_load_dwordx2 v[136:137], v63, s[52:55], s84 offen offset:1024 sc1\n"
    "buffer_load_dwordx2 v[138:139], v63, s[52:55], s84 offen offset:2048 sc1\n"
    "buffer_load_dwordx2 v[140:141], v63, s[52:55], s84 offen offset:3072 sc1\n"
    "buffer_load_dwordx4 a[144:147], v247, s[52:55], s83 offen offset:1024 sc1\n"
    "buffer_load_dwordx4 a[148:151], v247, s[52:55], s83 offen offset:1280 sc1\n"
    "buffer_load_dwordx4 a[152:155], v247, s[52:55], s83 offen offset:1536 sc1\n"
    "buffer_load_dwordx4 a[156:159], v247, s[52:55], s83 offen offset:1792 sc1\n"
    "s_waitcnt vmcnt(11)\n"
    "s_waitcnt lgkmcnt(6)\n"
    "v_mfma_f32_32x32x16_bf16 v[144:159], v[100:103], a[128:131], 0\n"
    "ds_read_b64_tr_b16 v[120:121], v250 offset:6144\n"
    "ds_read_b64_tr_b16 v[122:123], v250 offset:6656\n"
    "buffer_load_dwordx4 a[160:163], v247, s[52:55], s83 offen offset:2048 sc1\n"
    "v_mfma_f32_32x32x16_bf16 v[160:175], v[104:107], a[128:131], 0\n"
    "ds_read_b64_tr_b16 v[124:125], v249 offset:9216\n"
    "ds_read_b64_tr_b16 v[126:127], v249 offset:9728\n"
    "s_waitcnt lgkmcnt(6)\n"
    "s_waitcnt vmcnt(11)\n"
    "v_mfma_f32_32x32x16_bf16 v[144:159], v[108:111], a[132:135], v[144:159]\n"
    "ds_read_b64_tr_b16 v[128:129], v250 offset:9216\n"
    "ds_read_b64_tr_b16 v[130:131], v250 offset:9728\n"
    "buffer_load_dwordx4 a[164:167], v247, s[52:55], s83 offen offset:2304 sc1\n"
    "v_mfma_f32_32x32x16_bf16 v[160:175], v[112:115], a[132:135], v[160:175]\n"
    "ds_read_b64_tr_b16 v[100:101], v249 offset:12288\n"
    "ds_read_b64_tr_b16 v[102:103], v249 offset:12800\n"
    "ds_read_b64_tr_b16 a[0:1], v10\n"
    "ds_read_b64_tr_b16 a[2:3], v10 offset:1024\n"
    "s_waitcnt lgkmcnt(8)\n"
    "s_waitcnt vmcnt(11)\n"
    "v_mfma_f32_32x32x16_bf16 v[144:159], v[116:119], a[136:139], v[144:159]\n"
    "ds_read_b64_tr_b16 v[104:105], v250 offset:12288\n"
    "ds_read_b64_tr_b16 v[106:107], v250 offset:12800\n"
    "buffer_load_dwordx4 a[168:171], v247, s[52:55], s83 offen offset:2560 sc1\n"
    "v_mfma_f32_32x32x16_bf16 v[160:175], v[120:123], a[136:139], v[160:175]\n"
    "ds_read_b64_tr_b16 v[108:109], v249 offset:15360\n"
    "ds_read_b64_tr_b16 v[110:111], v249 offset:15872\n"
    "ds_read_b64_tr_b16 a[4:5], v11\n"
    "ds_read_b64_tr_b16 a[6:7], v11 offset:1024\n"
    "s_waitcnt lgkmcnt(10)\n"
    "s_waitcnt vmcnt(11)\n"
    "v_mfma_f32_32x32x16_bf16 v[144:159], v[124:127], a[140:143], v[144:159]\n"
    "ds_read_b64_tr_b16 v[112:113], v250 offset:15360\n"
    "ds_read_b64_tr_b16 v[114:115], v250 offset:15872\n"
    "buffer_load_dwordx4 a[172:175], v247, s[52:55], s83 offen offset:2816 sc1\n"
    "v_mfma_f32_32x32x16_bf16 v[160:175], v[128:131], a[140:143], v[160:175]\n"
    "ds_read_b64_tr_b16 v[116:117], v251\n"
    "ds_read_b64_tr_b16 v[118:119], v251 offset:512\n"
    "ds_read_b64_tr_b16 a[8:9], v10 offset:2048\n"
    "ds_read_b64_tr_b16 a[10:11], v10 offset:3072\n"
    "s_waitcnt lgkmcnt(10)\n"
    "s_waitcnt vmcnt(7)\n"
    "v_mfma_f32_32x32x16_bf16 v[144:159], v[100:103], a[144:147], v[144:159]\n"
    "ds_read_b64_tr_b16 v[120:121], v249 offset:18432\n"
    "ds_read_b64_tr_b16 v[122:123], v249 offset:18944\n"
    "buffer_load_dwordx4 a[176:179], v247, s[52:55], s83 offen offset:3072 sc1\n"
    "v_mfma_f32_32x32x16_bf16 v[160:175], v[104:107], a[144:147], v[160:175]\n"
    "ds_read_b64_tr_b16 v[124:125], v250 offset:18432\n"
    "ds_read_b64_tr_b16 v[126:127], v250 offset:18944\n"
    "ds_read_b64_tr_b16 a[12:13], v11 offset:2048\n"
    "ds_read_b64_tr_b16 a[14:15], v11 offset:3072\n"
    "s_waitcnt lgkmcnt(10)\n"
    "s_waitcnt vmcnt(7)\n"
    "v_mfma_f32_32x32x16_bf16 v[144:159], v[108:111], a[148:151], v[144:159]\n"
    "ds_read_b64_tr_b16 v[128:129], v249 offset:21504\n"
    "ds_read_b64_tr_b16 v[130:131], v249 offset:22016\n"
    "buffer_load_dwordx4 a[180:183], v247, s[52:55], s83 offen offset:3328 sc1\n"
    "v_mfma_f32_32x32x16_bf16 v[160:175], v[112:115], a[148:151], v[160:175]\n"
    "ds_read_b64_tr_b16 v[100:101], v250 offset:21504\n"
    "ds_read_b64_tr_b16 v[102:103], v250 offset:22016\n"
    "ds_read_b64_tr_b16 a[16:17], v10 offset:4096\n"
    "ds_read_b64_tr_b16 a[18:19], v10 offset:5120\n"
    "s_waitcnt lgkmcnt(10)\n"
    "v_mfma_f32_32x32x16_bf16 v[176:191], v[116:119], a[128:131], 0\n"
    "ds_read_b64_tr_b16 v[104:105], v249 offset:24576\n"
    "ds_read_b64_tr_b16 v[106:107], v249 offset:25088\n"
    "ds_read_b64_tr_b16 a[20:21], v11 offset:4096\n"
    "ds_read_b64_tr_b16 a[22:23], v11 offset:5120\n"
    "s_waitcnt vmcnt(7)\n"
    "v_mfma_f32_32x32x16_bf16 v[144:159], v[120:123], a[152:155], v[144:159]\n"
    "ds_read_b64_tr_b16 v[108:109], v250 offset:24576\n"
    "ds_read_b64_tr_b16 v[110:111], v250 offset:25088\n"
    "buffer_load_dwordx4 a[184:187], v247, s[52:55], s83 offen offset:3584 sc1\n"
    "s_waitcnt lgkmcnt(10)\n"
    "v_mfma_f32_32x32x16_bf16 v[160:175], v[124:127], a[152:155], v[160:175]\n"
    "ds_read_b64_tr_b16 v[112:113], v249 offset:27648\n"
    "ds_read_b64_tr_b16 v[114:115], v249 offset:28160\n"
    "ds_read_b64_tr_b16 a[24:25], v10 offset:6144\n"
    "ds_read_b64_tr_b16 a[26:27], v10 offset:7168\n"
    "s_waitcnt vmcnt(7)\n"
    "v_mfma_f32_32x32x16_bf16 v[144:159], v[128:131], a[156:159], v[144:159]\n"
    "ds_read_b64_tr_b16 v[116:117], v250 offset:27648\n"
    "ds_read_b64_tr_b16 v[118:119], v250 offset:28160\n"
    "buffer_load_dwordx4 a[188:191], v247, s[52:55], s83 offen offset:3840 sc1\n"
    "s_waitcnt lgkmcnt(10)\n"
    "v_mfma_f32_32x32x16_bf16 v[160:175], v[100:103], a[156:159], v[160:175]\n"
    "ds_read_b64_tr_b16 v[120:121], v249 offset:30720\n"
    "ds_read_b64_tr_b16 v[122:123], v249 offset:31232\n"
    "ds_read_b64_tr_b16 a[28:29], v11 offset:6144\n"
    "ds_read_b64_tr_b16 a[30:31], v11 offset:7168\n"
    "s_waitcnt vmcnt(7)\n"
    "v_mfma_f32_32x32x16_bf16 v[144:159], v[104:107], a[160:163], v[144:159]\n"
    "ds_read_b64_tr_b16 v[124:125], v250 offset:30720\n"
    "ds_read_b64_tr_b16 v[126:127], v250 offset:31232\n"
    "s_add_u32 s65, s83, 0x1000\n"
    "buffer_load_dwordx4 a[192:195], v247, s[52:55], s65 offen sc1\n"
    "s_waitcnt lgkmcnt(10)\n"
    "v_mfma_f32_32x32x16_bf16 v[160:175], v[108:111], a[160:163], v[160:175]\n"
    "ds_read_b64_tr_b16 v[128:129], v251 offset:3072\n"
    "ds_read_b64_tr_b16 v[130:131], v251 offset:3584\n"
    "ds_read_b64_tr_b16 a[32:33], v10 offset:8192\n"
    "ds_read_b64_tr_b16 a[34:35], v10 offset:9216\n"
    "s_waitcnt vmcnt(7)\n"
    "v_mfma_f32_32x32x16_bf16 v[144:159], v[112:115], a[164:167], v[144:159]\n"
    "ds_read_b64_tr_b16 v[100:101], v249 offset:33792\n"
    "ds_read_b64_tr_b16 v[102:103], v249 offset:34304\n"
    "buffer_load_dwordx4 a[196:199], v247, s[52:55], s65 offen offset:256 sc1\n"
    "s_waitcnt lgkmcnt(10)\n"
    "v_mfma_f32_32x32x16_bf16 v[160:175], v[116:119], a[164:167], v[160:175]\n"
    "ds_read_b64_tr_b16 v[104:105], v250 offset:33792\n"
    "ds_read_b64_tr_b16 v[106:107], v250 offset:34304\n"
    "ds_read_b64_tr_b16 a[36:37], v11 offset:8192\n"
    "ds_read_b64_tr_b16 a[38:39], v11 offset:9216\n"
    "s_waitcnt vmcnt(7)\n"
    "v_mfma_f32_32x32x16_bf16 v[144:159], v[120:123], a[168:171], v[144:159]\n"
    "ds_read_b64_tr_b16 v[108:109], v249 offset:36864\n"
    "ds_read_b64_tr_b16 v[110:111], v249 offset:37376\n"
    "buffer_load_dwordx4 a[200:203], v247, s[52:55], s65 offen offset:512 sc1\n"
    "s_waitcnt lgkmcnt(10)\n"
    "v_mfma_f32_32x32x16_bf16 v[160:175], v[124:127], a[168:171], v[160:175]\n"
    "ds_read_b64_tr_b16 v[112:113], v250 offset:36864\n"
    "ds_read_b64_tr_b16 v[114:115], v250 offset:37376\n"
    "ds_read_b64_tr_b16 a[40:41], v10 offset:10240\n"
    "ds_read_b64_tr_b16 a[42:43], v10 offset:11264\n"
    "v_mfma_f32_32x32x16_bf16 v[176:191], v[128:131], a[132:135], v[176:191]\n"
    "ds_read_b64_tr_b16 v[116:117], v249 offset:39936\n"
    "ds_read_b64_tr_b16 v[118:119], v249 offset:40448\n"
    "ds_read_b64_tr_b16 a[44:45], v11 offset:10240\n"
    "ds_read_b64_tr_b16 a[46:47], v11 offset:11264\n"
    "s_waitcnt lgkmcnt(12)\n"
    "s_waitcnt vmcnt(7)\n"
    "v_mfma_f32_32x32x16_bf16 v[144:159], v[100:103], a[172:175], v[144:159]\n"
    "ds_read_b64_tr_b16 v[120:121], v250 offset:39936\n"
    "ds_read_b64_tr_b16 v[122:123], v250 offset:40448\n"
    "buffer_load_dwordx4 a[204:207], v247, s[52:55], s65 offen offset:768 sc1\n"
    "v_mfma_f32_32x32x16_bf16 v[160:175], v[104:107], a[172:175], v[160:175]\n"
    "ds_read_b64_tr_b16 v[124:125], v249 offset:43008\n"
    "ds_read_b64_tr_b16 v[126:127], v249 offset:43520\n"
    "v_add_f32 v134, v134, v136\n"
    "v_add_f32 v135, v135, v137\n"
    "v_add_f32 v138, v138, v140\n"
    "v_add_f32 v139, v139, v141\n"
    "s_waitcnt lgkmcnt(10)\n"
    "s_waitcnt vmcnt(7)\n"
    "v_mfma_f32_32x32x16_bf16 v[144:159], v[108:111], a[176:179], v[144:159]\n"
    "ds_read_b64_tr_b16 v[128:129], v250 offset:43008\n"
    "ds_read_b64_tr_b16 v[130:131], v250 offset:43520\n"
    "buffer_load_dwordx4 a[208:211], v247, s[52:55], s65 offen offset:1024 sc1\n"
    "v_mfma_f32_32x32x16_bf16 v[160:175], v[112:115], a[176:179], v[160:175]\n"
    "ds_read_b64_tr_b16 v[100:101], v249 offset:46080\n"
    "ds_read_b64_tr_b16 v[102:103], v249 offset:46592\n"
    "v_add_f32 v136, v134, v138\n"
    "v_add_f32 v137, v135, v139\n"
    "v_add_f32 v134, v134, v138\n"
    "v_add_f32 v135, v135, v139\n"
    "s_waitcnt lgkmcnt(6)\n"
    "s_waitcnt vmcnt(7)\n"
    "v_mfma_f32_32x32x16_bf16 v[144:159], v[116:119], a[180:183], v[144:159]\n"
    "ds_read_b64_tr_b16 v[104:105], v250 offset:46080\n"
    "ds_read_b64_tr_b16 v[106:107], v250 offset:46592\n"
    "buffer_load_dwordx4 a[212:215], v247, s[52:55], s65 offen offset:1280 sc1\n"
    "v_mfma_f32_32x32x16_bf16 v[160:175], v[120:123], a[180:183], v[160:175]\n"
    "ds_read_b64_tr_b16 v[108:109], v251 offset:6144\n"
    "ds_read_b64_tr_b16 v[110:111], v251 offset:6656\n"
    "v_permlane32_swap_b32 v134, v136\n"
    "v_permlane32_swap_b32 v135, v137\n"
    "s_waitcnt lgkmcnt(6)\n"
    "s_waitcnt vmcnt(7)\n"
    "v_mfma_f32_32x32x16_bf16 v[144:159], v[124:127], a[184:187], v[144:159]\n"
    "ds_read_b64_tr_b16 v[112:113], v249 offset:49152\n"
    "ds_read_b64_tr_b16 v[114:115], v249 offset:49664\n"
    "buffer_load_dwordx4 a[216:219], v247, s[52:55], s65 offen offset:1536 sc1\n"
    "v_mfma_f32_32x32x16_bf16 v[160:175], v[128:131], a[184:187], v[160:175]\n"
    "ds_read_b64_tr_b16 v[116:117], v250 offset:49152\n"
    "ds_read_b64_tr_b16 v[118:119], v250 offset:49664\n"
    "v_add_f32 v134, v134, v136\n"
    "v_add_f32 v135, v135, v137\n"
    "v_mul_f32 v92, s8, v134\n"
    "v_mul_f32 v135, s8, v135\n"
    "s_waitcnt lgkmcnt(6)\n"
    "s_waitcnt vmcnt(7)\n"
    "v_mfma_f32_32x32x16_bf16 v[144:159], v[100:103], a[188:191], v[144:159]\n"
    "ds_read_b64_tr_b16 v[120:121], v249 offset:52224\n"
    "ds_read_b64_tr_b16 v[122:123], v249 offset:52736\n"
    "buffer_load_dwordx4 a[220:223], v247, s[52:55], s65 offen offset:1792 sc1\n"
    "v_mfma_f32_32x32x16_bf16 v[160:175], v[104:107], a[188:191], v[160:175]\n"
    "ds_read_b64_tr_b16 v[124:125], v250 offset:52224\n"
    "ds_read_b64_tr_b16 v[126:127], v250 offset:52736\n"
    "v_fma_f32 v135, -v92, v92, v135\n"
    "v_max_f32 v135, 0, v135\n"
    "v_add_f32 v135, 0x3727c5ac, v135\n"
    "s_waitcnt lgkmcnt(6)\n"
    "v_mfma_f32_32x32x16_bf16 v[176:191], v[108:111], a[136:139], v[176:191]\n"
    "ds_read_b64_tr_b16 v[128:129], v249 offset:55296\n"
    "ds_read_b64_tr_b16 v[130:131], v249 offset:55808\n"
    "v_rsq_f32 v94, v135\n"
    "s_waitcnt vmcnt(7)\n"
    "v_mfma_f32_32x32x16_bf16 v[144:159], v[112:115], a[192:195], v[144:159]\n"
    "ds_read_b64_tr_b16 v[100:101], v250 offset:55296\n"
    "ds_read_b64_tr_b16 v[102:103], v250 offset:55808\n"
    "buffer_load_dwordx4 a[224:227], v247, s[52:55], s65 offen offset:2048 sc1\n"
    "s_waitcnt lgkmcnt(6)\n"
    "v_mfma_f32_32x32x16_bf16 v[160:175], v[116:119], a[192:195], v[160:175]\n"
    "ds_read_b64_tr_b16 v[104:105], v249 offset:58368\n"
    "ds_read_b64_tr_b16 v[106:107], v249 offset:58880\n"
    "v_mul_f32 v94, s10, v94\n"
    "v_mul_f32 v96, v94, v92\n"
    "s_waitcnt vmcnt(7)\n"
    "v_mfma_f32_32x32x16_bf16 v[144:159], v[120:123], a[196:199], v[144:159]\n"
    "ds_read_b64_tr_b16 v[108:109], v250 offset:58368\n"
    "ds_read_b64_tr_b16 v[110:111], v250 offset:58880\n"
    "buffer_load_dwordx4 a[228:231], v247, s[52:55], s65 offen offset:2304 sc1\n"
    "s_waitcnt lgkmcnt(6)\n"
    "v_mfma_f32_32x32x16_bf16 v[160:175], v[124:127], a[196:199], v[160:175]\n"
    "ds_read_b64_tr_b16 v[112:113], v244 offset:12288\n"
    "ds_read_b64_tr_b16 v[114:115], v244 offset:12800\n"
    "ds_read_b128 v[76:79], v62 offset:512\n"
    "ds_read_b128 v[80:83], v62 offset:544\n"
    "ds_read_b128 v[84:87], v62 offset:576\n"
    "s_waitcnt vmcnt(7)\n"
    "v_mfma_f32_32x32x16_bf16 v[144:159], v[128:131], a[200:203], v[144:159]\n"
    "ds_read_b64_tr_b16 v[116:117], v245 offset:12288\n"
    "ds_read_b64_tr_b16 v[118:119], v245 offset:12800\n"
    "buffer_load_dwordx4 a[232:235], v247, s[52:55], s65 offen offset:2560 sc1\n"
    "s_waitcnt lgkmcnt(9)\n"
    "v_mfma_f32_32x32x16_bf16 v[160:175], v[100:103], a[200:203], v[160:175]\n"
    "ds_read_b64_tr_b16 v[120:121], v244 offset:15360\n"
    "ds_read_b64_tr_b16 v[122:123], v244 offset:15872\n"
    "s_waitcnt lgkmcnt(6)\n"
    "v_fma_f32 v192, -v96, v192, v76\n"
    "v_fma_f32 v193, -v96, v193, v77\n"
    "v_fma_f32 v194, -v96, v194, v78\n"
    "v_fma_f32 v195, -v96, v195, v79\n"
    "ds_read_b128 v[88:91], v62 offset:608\n"
    "s_waitcnt vmcnt(7)\n"
    "v_mfma_f32_32x32x16_bf16 v[144:159], v[104:107], a[204:207], v[144:159]\n"
    "ds_read_b64_tr_b16 v[124:125], v245 offset:15360\n"
    "ds_read_b64_tr_b16 v[126:127], v245 offset:15872\n"
    "buffer_load_dwordx4 a[236:239], v247, s[52:55], s65 offen offset:2816 sc1\n"
    "v_mfma_f32_32x32x16_bf16 v[160:175], v[108:111], a[204:207], v[160:175]\n"
    "ds_read_b64_tr_b16 v[128:129], v251 offset:9216\n"
    "ds_read_b64_tr_b16 v[130:131], v251 offset:9728\n"
    "s_waitcnt lgkmcnt(10)\n"
    "v_fma_f32 v196, -v96, v196, v80\n"
    "v_fma_f32 v197, -v96, v197, v81\n"
    "v_fma_f32 v198, -v96, v198, v82\n"
    "v_fma_f32 v199, -v96, v199, v83\n"
    "ds_read_b128 v[76:79], v62 offset:640\n"
    "s_waitcnt vmcnt(7)\n"
    "v_mfma_f32_32x32x16_bf16 v[144:159], v[112:115], a[208:211], v[144:159]\n"
    "ds_read_b64_tr_b16 v[100:101], v244 offset:18432\n"
    "ds_read_b64_tr_b16 v[102:103], v244 offset:18944\n"
    "buffer_load_dwordx4 a[240:243], v247, s[52:55], s65 offen offset:3072 sc1\n"
    "s_waitcnt lgkmcnt(8)\n"
    "v_mfma_f32_32x32x16_bf16 v[160:175], v[116:119], a[208:211], v[160:175]\n"
    "ds_read_b64_tr_b16 v[104:105], v245 offset:18432\n"
    "ds_read_b64_tr_b16 v[106:107], v245 offset:18944\n"
    "v_fma_f32 v200, -v96, v200, v84\n"
    "v_fma_f32 v201, -v96, v201, v85\n"
    "v_fma_f32 v202, -v96, v202, v86\n"
    "v_fma_f32 v203, -v96, v203, v87\n"
    "ds_read_b128 v[80:83], v62 offset:672\n"
    "s_waitcnt vmcnt(7)\n"
    "v_mfma_f32_32x32x16_bf16 v[144:159], v[120:123], a[212:215], v[144:159]\n"
    "ds_read_b64_tr_b16 v[108:109], v244 offset:21504\n"
    "ds_read_b64_tr_b16 v[110:111], v244 offset:22016\n"
    "buffer_load_dwordx4 a[244:247], v247, s[52:55], s65 offen offset:3328 sc1\n"
    "s_waitcnt lgkmcnt(8)\n"
    "v_mfma_f32_32x32x16_bf16 v[160:175], v[124:127], a[212:215], v[160:175]\n"
    "ds_read_b64_tr_b16 v[112:113], v245 offset:21504\n"
    "ds_read_b64_tr_b16 v[114:115], v245 offset:22016\n"
    "v_fma_f32 v204, -v96, v204, v88\n"
    "v_fma_f32 v205, -v96, v205, v89\n"
    "v_fma_f32 v206, -v96, v206, v90\n"
    "v_fma_f32 v207, -v96, v207, v91\n"
    "ds_read_b128 v[84:87], v62 offset:704\n"
    "v_mfma_f32_32x32x16_bf16 v[176:191], v[128:131], a[140:143], v[176:191]\n"
    "ds_read_b64_tr_b16 v[116:117], v244 offset:24576\n"
    "ds_read_b64_tr_b16 v[118:119], v244 offset:25088\n"
    "s_waitcnt lgkmcnt(12)\n"
    "v_fma_f32 v208, -v96, v208, v76\n"
    "v_fma_f32 v209, -v96, v209, v77\n"
    "v_fma_f32 v210, -v96, v210, v78\n"
    "v_fma_f32 v211, -v96, v211, v79\n"
    "ds_read_b128 v[88:91], v62 offset:736\n"
    "s_waitcnt lgkmcnt(9)\n"
    "s_waitcnt vmcnt(7)\n"
    "v_mfma_f32_32x32x16_bf16 v[144:159], v[100:103], a[216:219], v[144:159]\n"
    "ds_read_b64_tr_b16 v[120:121], v245 offset:24576\n"
    "ds_read_b64_tr_b16 v[122:123], v245 offset:25088\n"
    "buffer_load_dwordx4 a[248:251], v247, s[52:55], s65 offen offset:3584 sc1\n"
    "v_mfma_f32_32x32x16_bf16 v[160:175], v[104:107], a[216:219], v[160:175]\n"
    "ds_read_b64_tr_b16 v[124:125], v244 offset:27648\n"
    "ds_read_b64_tr_b16 v[126:127], v244 offset:28160\n"
    "s_waitcnt lgkmcnt(12)\n"
    "v_fma_f32 v212, -v96, v212, v80\n"
    "v_fma_f32 v213, -v96, v213, v81\n"
    "v_fma_f32 v214, -v96, v214, v82\n"
    "v_fma_f32 v215, -v96, v215, v83\n"
    "ds_read_b128 v[76:79], v62 offset:768\n"
    "s_waitcnt lgkmcnt(9)\n"
    "s_waitcnt vmcnt(7)\n"
    "v_mfma_f32_32x32x16_bf16 v[144:159], v[108:111], a[220:223], v[144:159]\n"
    "ds_read_b64_tr_b16 v[128:129], v245 offset:27648\n"
    "ds_read_b64_tr_b16 v[130:131], v245 offset:28160\n"
    "buffer_load_dwordx4 a[252:255], v247, s[52:55], s65 offen offset:3840 sc1\n"
    "v_mfma_f32_32x32x16_bf16 v[160:175], v[112:115], a[220:223], v[160:175]\n"
    "ds_read_b64_tr_b16 v[100:101], v244 offset:30720\n"
    "ds_read_b64_tr_b16 v[102:103], v244 offset:31232\n"
    "s_waitcnt lgkmcnt(12)\n"
    "v_fma_f32 v216, -v96, v216, v84\n"
    "v_fma_f32 v217, -v96, v217, v85\n"
    "v_fma_f32 v218, -v96, v218, v86\n"
    "v_fma_f32 v219, -v96, v219, v87\n"
    "ds_read_b128 v[80:83], v62 offset:800\n"
    "s_waitcnt lgkmcnt(8)\n"
    "s_waitcnt vmcnt(7)\n"
    "v_mfma_f32_32x32x16_bf16 v[144:159], v[116:119], a[224:227], v[144:159]\n"
    "ds_read_b64_tr_b16 v[104:105], v245 offset:30720\n"
    "ds_read_b64_tr_b16 v[106:107], v245 offset:31232\n"
    "s_and_b32 s75, s57, 3\n"
    "s_mul_i32 s75, s75, 0xa000\n"
    "s_mul_i32 s76, s59, 0x58000\n"
    "s_add_u32 s75, s75, s76\n"
    "s_lshl_b32 s76, s61, 7\n"
    "s_add_u32 s75, s75, s76\n"
    "s_add_u32 s75, s75, 0x30000\n"
    "v_min_u32 v61, 39, v23\n"
    "v_lshlrev_b32 v61, 10, v61\n"
    "s_cmp_eq_u32 s61, 3\n"
    "s_cselect_b32 s76, 0, 0x7fff0000\n"
    "v_add_u32 v61, s76, v61\n"
    "buffer_load_dword v252, v61, s[44:47], s75 offen\n"
    "v_mfma_f32_32x32x16_bf16 v[160:175], v[120:123], a[224:227], v[160:175]\n"
    "ds_read_b64_tr_b16 v[108:109], v251 offset:12288\n"
    "ds_read_b64_tr_b16 v[110:111], v251 offset:12800\n"
    "v_fma_f32 v220, -v96, v220, v88\n"
    "v_fma_f32 v221, -v96, v221, v89\n"
    "v_fma_f32 v222, -v96, v222, v90\n"
    "v_fma_f32 v223, -v96, v223, v91\n"
    "ds_read_b128 v[84:87], v62 offset:832\n"
    "s_waitcnt lgkmcnt(8)\n"
    "s_waitcnt vmcnt(7)\n"
    "v_mfma_f32_32x32x16_bf16 v[144:159], v[124:127], a[228:231], v[144:159]\n"
    "ds_read_b64_tr_b16 v[112:113], v244 offset:33792\n"
    "ds_read_b64_tr_b16 v[114:115], v244 offset:34304\n"
    "v_fma_f32 v224, -v96, v224, v76\n"
    "v_fma_f32 v225, -v96, v225, v77\n"
    "v_fma_f32 v226, -v96, v226, v78\n"
    "v_fma_f32 v227, -v96, v227, v79\n"
    "ds_read_b128 v[88:91], v62 offset:864\n"
    "v_mfma_f32_32x32x16_bf16 v[160:175], v[128:131], a[228:231], v[160:175]\n"
    "ds_read_b64_tr_b16 v[116:117], v245 offset:33792\n"
    "ds_read_b64_tr_b16 v[118:119], v245 offset:34304\n"
    "s_waitcnt lgkmcnt(10)\n"
    "v_fma_f32 v228, -v96, v228, v80\n"
    "v_fma_f32 v229, -v96, v229, v81\n"
    "v_fma_f32 v230, -v96, v230, v82\n"
    "v_fma_f32 v231, -v96, v231, v83\n"
    "s_waitcnt lgkmcnt(8)\n"
    "s_waitcnt vmcnt(6)\n"
    "v_mfma_f32_32x32x16_bf16 v[144:159], v[100:103], a[232:235], v[144:159]\n"
    "ds_read_b64_tr_b16 v[120:121], v244 offset:36864\n"
    "ds_read_b64_tr_b16 v[122:123], v244 offset:37376\n"
    "s_waitcnt lgkmcnt(7)\n"
    "v_fma_f32 v232, -v96, v232, v84\n"
    "v_fma_f32 v233, -v96, v233, v85\n"
    "v_fma_f32 v234, -v96, v234, v86\n"
    "v_fma_f32 v235, -v96, v235, v87\n"
    "v_mfma_f32_32x32x16_bf16 v[160:175], v[104:107], a[232:235], v[160:175]\n"
    "ds_read_b64_tr_b16 v[124:125], v245 offset:36864\n"
    "ds_read_b64_tr_b16 v[126:127], v245 offset:37376\n"
    "s_waitcnt lgkmcnt(6)\n"
    "v_fma_f32 v236, -v96, v236, v88\n"
    "v_fma_f32 v237, -v96, v237, v89\n"
    "v_fma_f32 v238, -v96, v238, v90\n"
    "v_fma_f32 v239, -v96, v239, v91\n"
    "v_mfma_f32_32x32x16_bf16 v[176:191], v[108:111], a[144:147], v[176:191]\n"
    "ds_read_b64_tr_b16 v[128:129], v244 offset:39936\n"
    "ds_read_b64_tr_b16 v[130:131], v244 offset:40448\n"
    "s_add_u32 m0, s82, 0\n"
    "s_add_u32 s66, s81, 0x18000\n"
    "buffer_load_dwordx4 v248, s[44:47], s66 offen lds\n"
    "s_waitcnt vmcnt(6)\n"
    "v_mfma_f32_32x32x16_bf16 v[144:159], v[112:115], a[236:239], v[144:159]\n"
    "ds_read_b64_tr_b16 v[100:101], v245 offset:39936\n"
    "ds_read_b64_tr_b16 v[102:103], v245 offset:40448\n"
    "s_addk_i32 s66, 7168\n"
    "buffer_load_dwordx4 v248, s[44:47], s66 offen offset:1024 lds\n"
    "s_waitcnt lgkmcnt(6)\n"
    "v_mfma_f32_32x32x16_bf16 v[160:175], v[116:119], a[236:239], v[160:175]\n"
    "ds_read_b64_tr_b16 v[104:105], v244 offset:43008\n"
    "ds_read_b64_tr_b16 v[106:107], v244 offset:43520\n"
    "s_addk_i32 s66, 7168\n"
    "buffer_load_dwordx4 v248, s[44:47], s66 offen offset:2048 lds\n"
    "s_waitcnt vmcnt(7)\n"
    "v_mfma_f32_32x32x16_bf16 v[144:159], v[120:123], a[240:243], v[144:159]\n"
    "ds_read_b64_tr_b16 v[108:109], v245 offset:43008\n"
    "ds_read_b64_tr_b16 v[110:111], v245 offset:43520\n"
    "s_addk_i32 s66, 7168\n"
    "buffer_load_dwordx4 v248, s[44:47], s66 offen offset:3072 lds\n"
    "s_waitcnt lgkmcnt(6)\n"
    "v_mfma_f32_32x32x16_bf16 v[160:175], v[124:127], a[240:243], v[160:175]\n"
    "ds_read_b64_tr_b16 v[112:113], v244 offset:46080\n"
    "ds_read_b64_tr_b16 v[114:115], v244 offset:46592\n"
    "s_addk_i32 m0, 4096\n"
    "s_addk_i32 s66, 11264\n"
    "buffer_load_dwordx4 v248, s[44:47], s66 offen lds\n"
    "s_waitcnt vmcnt(8)\n"
    "v_mfma_f32_32x32x16_bf16 v[144:159], v[128:131], a[244:247], v[144:159]\n"
    "ds_read_b64_tr_b16 v[116:117], v245 offset:46080\n"
    "ds_read_b64_tr_b16 v[118:119], v245 offset:46592\n"
    "s_addk_i32 s66, 7168\n"
    "buffer_load_dwordx4 v248, s[44:47], s66 offen offset:1024 lds\n"
    "s_waitcnt lgkmcnt(6)\n"
    "v_mfma_f32_32x32x16_bf16 v[160:175], v[100:103], a[244:247], v[160:175]\n"
    "ds_read_b64_tr_b16 v[120:121], v251 offset:15360\n"
    "ds_read_b64_tr_b16 v[122:123], v251 offset:15872\n"
    "s_addk_i32 s66, 7168\n"
    "buffer_load_dwordx4 v248, s[44:47], s66 offen offset:2048 lds\n"
    "s_waitcnt vmcnt(9)\n"
    "v_mfma_f32_32x32x16_bf16 v[144:159], v[104:107], a[248:251], v[144:159]\n"
    "ds_read_b64_tr_b16 v[124:125], v251 offset:18432\n"
    "ds_read_b64_tr_b16 v[126:127], v251 offset:18944\n"
    "s_addk_i32 s66, 7168\n"
    "buffer_load_dwordx4 v248, s[44:47], s66 offen offset:3072 lds\n"
    "s_waitcnt lgkmcnt(6)\n"
    "v_mfma_f32_32x32x16_bf16 v[160:175], v[108:111], a[248:251], v[160:175]\n"
    "ds_read_b64_tr_b16 v[128:129], v251 offset:21504\n"
    "ds_read_b64_tr_b16 v[130:131], v251 offset:22016\n"
    "s_addk_i32 m0, 4096\n"
    "s_addk_i32 s66, 11264\n"
    "buffer_load_dwordx4 v248, s[44:47], s66 offen lds\n"
    "s_waitcnt vmcnt(10)\n"
    "v_mfma_f32_32x32x16_bf16 v[144:159], v[112:115], a[252:255], v[144:159]\n"
    "ds_read_b64_tr_b16 v[100:101], v251 offset:24576\n"
    "ds_read_b64_tr_b16 v[102:103], v251 offset:25088\n"
    "s_addk_i32 s66, 7168\n"
    "buffer_load_dwordx4 v248, s[44:47], s66 offen offset:1024 lds\n"
    "s_waitcnt lgkmcnt(6)\n"
    "v_mfma_f32_32x32x16_bf16 v[160:175], v[116:119], a[252:255], v[160:175]\n"
    "ds_read_b64_tr_b16 v[104:105], v251 offset:27648\n"
    "ds_read_b64_tr_b16 v[106:107], v251 offset:28160\n"
    "s_addk_i32 s66, 7168\n"
    "buffer_load_dwordx4 v248, s[44:47], s66 offen offset:2048 lds\n"
    "v_mfma_f32_32x32x16_bf16 v[176:191], v[120:123], a[148:151], v[176:191]\n"
    "ds_read_b64_tr_b16 v[108:109], v251 offset:30720\n"
    "ds_read_b64_tr_b16 v[110:111], v251 offset:31232\n"
    "s_addk_i32 s66, 7168\n"
    "buffer_load_dwordx4 v248, s[44:47], s66 offen offset:3072 lds\n"
    "s_waitcnt lgkmcnt(6)\n"
    "v_mfma_f32_32x32x16_bf16 v[176:191], v[124:127], a[152:155], v[176:191]\n"
    "ds_read_b64_tr_b16 v[112:113], v251 offset:33792\n"
    "ds_read_b64_tr_b16 v[114:115], v251 offset:34304\n"
    "v_pk_fma_f32 v[144:145], v[144:145], v[94:95], v[192:193] op_sel_hi:[1,0,1]\n"
    "v_pk_fma_f32 v[146:147], v[146:147], v[94:95], v[194:195] op_sel_hi:[1,0,1]\n"
    "v_pk_fma_f32 v[148:149], v[148:149], v[94:95], v[196:197] op_sel_hi:[1,0,1]\n"
    "v_pk_fma_f32 v[150:151], v[150:151], v[94:95], v[198:199] op_sel_hi:[1,0,1]\n"
    "v_mfma_f32_32x32x16_bf16 v[176:191], v[128:131], a[156:159], v[176:191]\n"
    "ds_read_b64_tr_b16 v[116:117], v251 offset:36864\n"
    "ds_read_b64_tr_b16 v[118:119], v251 offset:37376\n"
    "v_exp_f32 v64, v144\n"
    "s_waitcnt lgkmcnt(6)\n"
    "v_mfma_f32_32x32x16_bf16 v[176:191], v[100:103], a[160:163], v[176:191]\n"
    "ds_read_b64_tr_b16 v[120:121], v251 offset:39936\n"
    "ds_read_b64_tr_b16 v[122:123], v251 offset:40448\n"
    "v_exp_f32 v65, v145\n"
    "v_exp_f32 v66, v146\n"
    "v_mfma_f32_32x32x16_bf16 v[176:191], v[104:107], a[164:167], v[176:191]\n"
    "ds_read_b64_tr_b16 v[124:125], v251 offset:43008\n"
    "ds_read_b64_tr_b16 v[126:127], v251 offset:43520\n"
    "v_exp_f32 v67, v147\n"
    "v_exp_f32 v68, v148\n"
    "s_waitcnt lgkmcnt(6)\n"
    "v_mfma_f32_32x32x16_bf16 v[176:191], v[108:111], a[168:171], v[176:191]\n"
    "ds_read_b64_tr_b16 v[128:129], v251 offset:46080\n"
    "ds_read_b64_tr_b16 v[130:131], v251 offset:46592\n"
    "v_exp_f32 v69, v149\n"
    "v_exp_f32 v70, v150\n"
    "v_exp_f32 v71, v151\n"
    "v_pk_fma_f32 v[64:65], v[64:65], s[20:21], s[20:21] op_sel_hi:[1,0,0]\n"
    "v_pk_fma_f32 v[66:67], v[66:67], s[20:21], s[20:21] op_sel_hi:[1,0,0]\n"
    "v_pk_fma_f32 v[68:69], v[68:69], s[20:21], s[20:21] op_sel_hi:[1,0,0]\n"
    "v_pk_fma_f32 v[70:71], v[70:71], s[20:21], s[20:21] op_sel_hi:[1,0,0]\n"
    "v_pk_fma_f32 v[64:65], v[64:65], v[64:65], s[20:21] op_sel:[0,0,1]\n"
    "v_pk_fma_f32 v[66:67], v[66:67], v[66:67], s[20:21] op_sel:[0,0,1]\n"
    "v_pk_fma_f32 v[68:69], v[68:69], v[68:69], s[20:21] op_sel:[0,0,1]\n"
    "v_pk_fma_f32 v[70:71], v[70:71], v[70:71], s[20:21] op_sel:[0,0,1]\n"
    "v_pk_fma_f32 v[152:153], v[152:153], v[94:95], v[200:201] op_sel_hi:[1,0,1]\n"
    "v_pk_fma_f32 v[154:155], v[154:155], v[94:95], v[202:203] op_sel_hi:[1,0,1]\n"
    "v_pk_fma_f32 v[156:157], v[156:157], v[94:95], v[204:205] op_sel_hi:[1,0,1]\n"
    "v_pk_fma_f32 v[158:159], v[158:159], v[94:95], v[206:207] op_sel_hi:[1,0,1]\n"
    "v_mfma_f32_32x32x16_bf16 v[176:191], v[112:115], a[172:175], v[176:191]\n"
    "ds_read_b64_tr_b16 v[100:101], v251 offset:49152\n"
    "ds_read_b64_tr_b16 v[102:103], v251 offset:49664\n"
    "v_rcp_f32 v64, v64\n"
    "v_exp_f32 v76, v152\n"
    "s_waitcnt lgkmcnt(6)\n"
    "v_mfma_f32_32x32x16_bf16 v[176:191], v[116:119], a[176:179], v[176:191]\n"
    "ds_read_b64_tr_b16 v[104:105], v251 offset:52224\n"
    "ds_read_b64_tr_b16 v[106:107], v251 offset:52736\n"
    "v_rcp_f32 v65, v65\n"
    "v_exp_f32 v77, v153\n"
    "v_rcp_f32 v66, v66\n"
    "v_mfma_f32_32x32x16_bf16 v[176:191], v[120:123], a[180:183], v[176:191]\n"
    "ds_read_b64_tr_b16 v[108:109], v251 offset:55296\n"
    "ds_read_b64_tr_b16 v[110:111], v251 offset:55808\n"
    "v_exp_f32 v78, v154\n"
    "v_rcp_f32 v67, v67\n"
    "s_waitcnt lgkmcnt(6)\n"
    "v_mfma_f32_32x32x16_bf16 v[176:191], v[124:127], a[184:187], v[176:191]\n"
    "ds_read_b64_tr_b16 v[112:113], v251 offset:58368\n"
    "ds_read_b64_tr_b16 v[114:115], v251 offset:58880\n"
    "v_exp_f32 v79, v155\n"
    "v_rcp_f32 v68, v68\n"
    "v_exp_f32 v80, v156\n"
    "v_mfma_f32_32x32x16_bf16 v[176:191], v[128:131], a[188:191], v[176:191]\n"
    "ds_read_b64_tr_b16 v[116:117], v246 offset:12288\n"
    "ds_read_b64_tr_b16 v[118:119], v246 offset:12800\n"
    "v_rcp_f32 v69, v69\n"
    "v_exp_f32 v81, v157\n"
    "v_rcp_f32 v70, v70\n"
    "s_waitcnt lgkmcnt(6)\n"
    "v_mfma_f32_32x32x16_bf16 v[176:191], v[100:103], a[192:195], v[176:191]\n"
    "ds_read_b64_tr_b16 v[120:121], v246 offset:15360\n"
    "ds_read_b64_tr_b16 v[122:123], v246 offset:15872\n"
    "v_exp_f32 v82, v158\n"
    "v_rcp_f32 v71, v71\n"
    "v_exp_f32 v83, v159\n"
    "v_pk_add_f32 v[64:65], v[64:65], s[22:23] op_sel_hi:[1,0] neg_lo:[1,0] neg_hi:[1,0]\n"
    "v_pk_add_f32 v[66:67], v[66:67], s[22:23] op_sel_hi:[1,0] neg_lo:[1,0] neg_hi:[1,0]\n"
    "v_pk_add_f32 v[68:69], v[68:69], s[22:23] op_sel_hi:[1,0] neg_lo:[1,0] neg_hi:[1,0]\n"
    "v_pk_add_f32 v[70:71], v[70:71], s[22:23] op_sel_hi:[1,0] neg_lo:[1,0] neg_hi:[1,0]\n"
    "v_pk_mul_f32 v[144:145], v[144:145], v[64:65]\n"
    "v_pk_mul_f32 v[146:147], v[146:147], v[66:67]\n"
    "v_pk_mul_f32 v[148:149], v[148:149], v[68:69]\n"
    "v_pk_mul_f32 v[150:151], v[150:151], v[70:71]\n"
    "v_pk_fma_f32 v[76:77], v[76:77], s[20:21], s[20:21] op_sel_hi:[1,0,0]\n"
    "v_pk_fma_f32 v[78:79], v[78:79], s[20:21], s[20:21] op_sel_hi:[1,0,0]\n"
    "v_pk_fma_f32 v[80:81], v[80:81], s[20:21], s[20:21] op_sel_hi:[1,0,0]\n"
    "v_pk_fma_f32 v[82:83], v[82:83], s[20:21], s[20:21] op_sel_hi:[1,0,0]\n"
    "v_pk_fma_f32 v[76:77], v[76:77], v[76:77], s[20:21] op_sel:[0,0,1]\n"
    "v_pk_fma_f32 v[78:79], v[78:79], v[78:79], s[20:21] op_sel:[0,0,1]\n"
    "v_pk_fma_f32 v[80:81], v[80:81], v[80:81], s[20:21] op_sel:[0,0,1]\n"
    "v_pk_fma_f32 v[82:83], v[82:83], v[82:83], s[20:21] op_sel:[0,0,1]\n"
    "v_pk_fma_f32 v[160:161], v[160:161], v[94:95], v[208:209] op_sel_hi:[1,0,1]\n"
    "v_pk_fma_f32 v[162:163], v[162:163], v[94:95], v[210:211] op_sel_hi:[1,0,1]\n"
    "v_pk_fma_f32 v[164:165], v[164:165], v[94:95], v[212:213] op_sel_hi:[1,0,1]\n"
    "v_pk_fma_f32 v[166:167], v[166:167], v[94:95], v[214:215] op_sel_hi:[1,0,1]\n"
    "v_mfma_f32_32x32x16_bf16 v[176:191], v[104:107], a[196:199], v[176:191]\n"
    "ds_read_b64_tr_b16 v[124:125], v246 offset:18432\n"
    "ds_read_b64_tr_b16 v[126:127], v246 offset:18944\n"
    "v_cvt_pk_bf16_f32 v72, v144, v145\n"
    "v_cvt_pk_bf16_f32 v73, v146, v147\n"
    "v_cvt_pk_bf16_f32 v74, v148, v149\n"
    "v_cvt_pk_bf16_f32 v75, v150, v151\n"
    "buffer_store_dwordx4 v[72:75], v247, s[52:55], s13 offen sc1\n"
    "v_rcp_f32 v76, v76\n"
    "v_exp_f32 v64, v160\n"
    "s_waitcnt lgkmcnt(6)\n"
    "v_mfma_f32_32x32x16_bf16 v[176:191], v[108:111], a[200:203], v[176:191]\n"
    "ds_read_b64_tr_b16 v[128:129], v246 offset:21504\n"
    "ds_read_b64_tr_b16 v[130:131], v246 offset:22016\n"
    "v_rcp_f32 v77, v77\n"
    "v_exp_f32 v65, v161\n"
    "v_mfma_f32_32x32x16_bf16 v[176:191], v[112:115], a[204:207], v[176:191]\n"
    "ds_read_b64_tr_b16 v[100:101], v246 offset:24576\n"
    "ds_read_b64_tr_b16 v[102:103], v246 offset:25088\n"
    "v_rcp_f32 v78, v78\n"
    "v_exp_f32 v66, v162\n"
    "v_rcp_f32 v79, v79\n"
    "s_waitcnt lgkmcnt(6)\n"
    "v_mfma_f32_32x32x16_bf16 v[176:191], v[116:119], a[208:211], v[176:191]\n"
    "ds_read_b64_tr_b16 v[104:105], v246 offset:27648\n"
    "ds_read_b64_tr_b16 v[106:107], v246 offset:28160\n"
    "v_exp_f32 v67, v163\n"
    "v_rcp_f32 v80, v80\n"
    "v_mfma_f32_32x32x16_bf16 v[176:191], v[120:123], a[212:215], v[176:191]\n"
    "ds_read_b64_tr_b16 v[108:109], v246 offset:30720\n"
    "ds_read_b64_tr_b16 v[110:111], v246 offset:31232\n"
    "v_exp_f32 v68, v164\n"
    "v_rcp_f32 v81, v81\n"
    "v_exp_f32 v69, v165\n"
    "s_waitcnt lgkmcnt(6)\n"
    "v_mfma_f32_32x32x16_bf16 v[176:191], v[124:127], a[216:219], v[176:191]\n"
    "ds_read_b64_tr_b16 v[112:113], v246 offset:33792\n"
    "ds_read_b64_tr_b16 v[114:115], v246 offset:34304\n"
    "v_rcp_f32 v82, v82\n"
    "v_exp_f32 v70, v166\n"
    "v_rcp_f32 v83, v83\n"
    "v_exp_f32 v71, v167\n"
    "v_pk_add_f32 v[76:77], v[76:77], s[22:23] op_sel_hi:[1,0] neg_lo:[1,0] neg_hi:[1,0]\n"
    "v_pk_add_f32 v[78:79], v[78:79], s[22:23] op_sel_hi:[1,0] neg_lo:[1,0] neg_hi:[1,0]\n"
    "v_pk_add_f32 v[80:81], v[80:81], s[22:23] op_sel_hi:[1,0] neg_lo:[1,0] neg_hi:[1,0]\n"
    "v_pk_add_f32 v[82:83], v[82:83], s[22:23] op_sel_hi:[1,0] neg_lo:[1,0] neg_hi:[1,0]\n"
    "v_pk_mul_f32 v[152:153], v[152:153], v[76:77]\n"
    "v_pk_mul_f32 v[154:155], v[154:155], v[78:79]\n"
    "v_pk_mul_f32 v[156:157], v[156:157], v[80:81]\n"
    "v_pk_mul_f32 v[158:159], v[158:159], v[82:83]\n"
    "v_pk_fma_f32 v[64:65], v[64:65], s[20:21], s[20:21] op_sel_hi:[1,0,0]\n"
    "v_pk_fma_f32 v[66:67], v[66:67], s[20:21], s[20:21] op_sel_hi:[1,0,0]\n"
    "v_pk_fma_f32 v[68:69], v[68:69], s[20:21], s[20:21] op_sel_hi:[1,0,0]\n"
    "v_pk_fma_f32 v[70:71], v[70:71], s[20:21], s[20:21] op_sel_hi:[1,0,0]\n"
    "v_pk_fma_f32 v[64:65], v[64:65], v[64:65], s[20:21] op_sel:[0,0,1]\n"
    "v_pk_fma_f32 v[66:67], v[66:67], v[66:67], s[20:21] op_sel:[0,0,1]\n"
    "v_pk_fma_f32 v[68:69], v[68:69], v[68:69], s[20:21] op_sel:[0,0,1]\n"
    "v_pk_fma_f32 v[70:71], v[70:71], v[70:71], s[20:21] op_sel:[0,0,1]\n"
    "v_pk_fma_f32 v[168:169], v[168:169], v[94:95], v[216:217] op_sel_hi:[1,0,1]\n"
    "v_pk_fma_f32 v[170:171], v[170:171], v[94:95], v[218:219] op_sel_hi:[1,0,1]\n"
    "v_pk_fma_f32 v[172:173], v[172:173], v[94:95], v[220:221] op_sel_hi:[1,0,1]\n"
    "v_pk_fma_f32 v[174:175], v[174:175], v[94:95], v[222:223] op_sel_hi:[1,0,1]\n"
    "v_mfma_f32_32x32x16_bf16 v[176:191], v[128:131], a[220:223], v[176:191]\n"
    "ds_read_b64_tr_b16 v[116:117], v246 offset:36864\n"
    "ds_read_b64_tr_b16 v[118:119], v246 offset:37376\n"
    "v_cvt_pk_bf16_f32 v240, v152, v153\n"
    "v_cvt_pk_bf16_f32 v241, v154, v155\n"
    "v_cvt_pk_bf16_f32 v242, v156, v157\n"
    "v_cvt_pk_bf16_f32 v243, v158, v159\n"
    "buffer_store_dwordx4 v[240:243], v247, s[52:55], s13 offen offset:256 sc1\n"
    "v_rcp_f32 v64, v64\n"
    "s_waitcnt lgkmcnt(6)\n"
    "v_mfma_f32_32x32x16_bf16 v[176:191], v[100:103], a[224:227], v[176:191]\n"
    "ds_read_b64_tr_b16 v[120:121], v246 offset:39936\n"
    "ds_read_b64_tr_b16 v[122:123], v246 offset:40448\n"
    "v_exp_f32 v76, v168\n"
    "v_rcp_f32 v65, v65\n"
    "v_exp_f32 v77, v169\n"
    "v_mfma_f32_32x32x16_bf16 v[176:191], v[104:107], a[228:231], v[176:191]\n"
    "ds_read_b64_tr_b16 v[124:125], v246 offset:43008\n"
    "ds_read_b64_tr_b16 v[126:127], v246 offset:43520\n"
    "v_rcp_f32 v66, v66\n"
    "v_exp_f32 v78, v170\n"
    "s_waitcnt lgkmcnt(6)\n"
    "v_mfma_f32_32x32x16_bf16 v[176:191], v[108:111], a[232:235], v[176:191]\n"
    "ds_read_b64_tr_b16 v[128:129], v246 offset:46080\n"
    "ds_read_b64_tr_b16 v[130:131], v246 offset:46592\n"
    "v_rcp_f32 v67, v67\n"
    "v_exp_f32 v79, v171\n"
    "v_rcp_f32 v68, v68\n"
    "v_mfma_f32_32x32x16_bf16 v[176:191], v[112:115], a[236:239], v[176:191]\n"
    "v_exp_f32 v80, v172\n"
    "v_rcp_f32 v69, v69\n"
    "s_waitcnt lgkmcnt(4)\n"
    "v_mfma_f32_32x32x16_bf16 v[176:191], v[116:119], a[240:243], v[176:191]\n"
    "v_exp_f32 v81, v173\n"
    "v_rcp_f32 v70, v70\n"
    "v_exp_f32 v82, v174\n"
    "v_rcp_f32 v71, v71\n"
    "v_exp_f32 v83, v175\n"
    "v_pk_add_f32 v[64:65], v[64:65], s[22:23] op_sel_hi:[1,0] neg_lo:[1,0] neg_hi:[1,0]\n"
    "v_pk_add_f32 v[66:67], v[66:67], s[22:23] op_sel_hi:[1,0] neg_lo:[1,0] neg_hi:[1,0]\n"
    "v_pk_add_f32 v[68:69], v[68:69], s[22:23] op_sel_hi:[1,0] neg_lo:[1,0] neg_hi:[1,0]\n"
    "v_pk_add_f32 v[70:71], v[70:71], s[22:23] op_sel_hi:[1,0] neg_lo:[1,0] neg_hi:[1,0]\n"
    "v_pk_mul_f32 v[160:161], v[160:161], v[64:65]\n"
    "v_pk_mul_f32 v[162:163], v[162:163], v[66:67]\n"
    "v_pk_mul_f32 v[164:165], v[164:165], v[68:69]\n"
    "v_pk_mul_f32 v[166:167], v[166:167], v[70:71]\n"
    "v_pk_fma_f32 v[76:77], v[76:77], s[20:21], s[20:21] op_sel_hi:[1,0,0]\n"
    "v_pk_fma_f32 v[78:79], v[78:79], s[20:21], s[20:21] op_sel_hi:[1,0,0]\n"
    "v_pk_fma_f32 v[80:81], v[80:81], s[20:21], s[20:21] op_sel_hi:[1,0,0]\n"
    "v_pk_fma_f32 v[82:83], v[82:83], s[20:21], s[20:21] op_sel_hi:[1,0,0]\n"
    "v_pk_fma_f32 v[76:77], v[76:77], v[76:77], s[20:21] op_sel:[0,0,1]\n"
    "v_pk_fma_f32 v[78:79], v[78:79], v[78:79], s[20:21] op_sel:[0,0,1]\n"
    "v_pk_fma_f32 v[80:81], v[80:81], v[80:81], s[20:21] op_sel:[0,0,1]\n"
    "v_pk_fma_f32 v[82:83], v[82:83], v[82:83], s[20:21] op_sel:[0,0,1]\n"
    "v_mfma_f32_32x32x16_bf16 v[176:191], v[120:123], a[244:247], v[176:191]\n"
    "v_cvt_pk_bf16_f32 v72, v160, v161\n"
    "s_waitcnt lgkmcnt(0)\n"
    "v_mfma_f32_32x32x16_bf16 v[176:191], v[124:127], a[248:251], v[176:191]\n"
    "v_cvt_pk_bf16_f32 v73, v162, v163\n"
    "v_cvt_pk_bf16_f32 v74, v164, v165\n"
    "v_cvt_pk_bf16_f32 v75, v166, v167\n"
    "buffer_store_dwordx4 v[72:75], v247, s[52:55], s13 offen offset:512 sc1\n"
    "v_rcp_f32 v76, v76\n"
    "v_rcp_f32 v77, v77\n"
    "v_rcp_f32 v78, v78\n"
    "v_mfma_f32_32x32x16_bf16 v[176:191], v[128:131], a[252:255], v[176:191]\n"
    "v_rcp_f32 v79, v79\n"
    "v_rcp_f32 v80, v80\n"
    "v_rcp_f32 v81, v81\n"
    "v_rcp_f32 v82, v82\n"
    "v_rcp_f32 v83, v83\n"
    "v_pk_add_f32 v[76:77], v[76:77], s[22:23] op_sel_hi:[1,0] neg_lo:[1,0] neg_hi:[1,0]\n"
    "v_pk_add_f32 v[78:79], v[78:79], s[22:23] op_sel_hi:[1,0] neg_lo:[1,0] neg_hi:[1,0]\n"
    "v_pk_add_f32 v[80:81], v[80:81], s[22:23] op_sel_hi:[1,0] neg_lo:[1,0] neg_hi:[1,0]\n"
    "v_pk_add_f32 v[82:83], v[82:83], s[22:23] op_sel_hi:[1,0] neg_lo:[1,0] neg_hi:[1,0]\n"
    "v_pk_mul_f32 v[168:169], v[168:169], v[76:77]\n"
    "v_pk_mul_f32 v[170:171], v[170:171], v[78:79]\n"
    "v_pk_mul_f32 v[172:173], v[172:173], v[80:81]\n"
    "v_pk_mul_f32 v[174:175], v[174:175], v[82:83]\n"
    "v_cvt_pk_bf16_f32 v240, v168, v169\n"
    "v_cvt_pk_bf16_f32 v241, v170, v171\n"
    "v_cvt_pk_bf16_f32 v242, v172, v173\n"
    "v_cvt_pk_bf16_f32 v243, v174, v175\n"
    "buffer_store_dwordx4 v[240:243], v247, s[52:55], s13 offen offset:768 sc1\n"
    "s_waitcnt vmcnt(4)\n"
    "s_cmp_lg_u32 s63, 3\n"
    "s_cbranch_scc1 .Lo15_%=\n"
    "v_pk_fma_f32 v[176:177], v[176:177], v[94:95], v[224:225] op_sel_hi:[1,0,1]\n"
    "v_pk_fma_f32 v[178:179], v[178:179], v[94:95], v[226:227] op_sel_hi:[1,0,1]\n"
    "v_pk_fma_f32 v[180:181], v[180:181], v[94:95], v[228:229] op_sel_hi:[1,0,1]\n"
    "v_pk_fma_f32 v[182:183], v[182:183], v[94:95], v[230:231] op_sel_hi:[1,0,1]\n"
    "v_exp_f32 v64, v176\n"
    "v_exp_f32 v65, v177\n"
    "v_exp_f32 v66, v178\n"
    "v_exp_f32 v67, v179\n"
    "v_exp_f32 v68, v180\n"
    "v_exp_f32 v69, v181\n"
    "v_exp_f32 v70, v182\n"
    "v_exp_f32 v71, v183\n"
    "v_pk_fma_f32 v[64:65], v[64:65], s[20:21], s[20:21] op_sel_hi:[1,0,0]\n"
    "v_pk_fma_f32 v[66:67], v[66:67], s[20:21], s[20:21] op_sel_hi:[1,0,0]\n"
    "v_pk_fma_f32 v[68:69], v[68:69], s[20:21], s[20:21] op_sel_hi:[1,0,0]\n"
    "v_pk_fma_f32 v[70:71], v[70:71], s[20:21], s[20:21] op_sel_hi:[1,0,0]\n"
    "v_pk_fma_f32 v[64:65], v[64:65], v[64:65], s[20:21] op_sel:[0,0,1]\n"
    "v_pk_fma_f32 v[66:67], v[66:67], v[66:67], s[20:21] op_sel:[0,0,1]\n"
    "v_pk_fma_f32 v[68:69], v[68:69], v[68:69], s[20:21] op_sel:[0,0,1]\n"
    "v_pk_fma_f32 v[70:71], v[70:71], v[70:71], s[20:21] op_sel:[0,0,1]\n"
    "v_pk_fma_f32 v[184:185], v[184:185], v[94:95], v[232:233] op_sel_hi:[1,0,1]\n"
    "v_pk_fma_f32 v[186:187], v[186:187], v[94:95], v[234:235] op_sel_hi:[1,0,1]\n"
    "v_pk_fma_f32 v[188:189], v[188:189], v[94:95], v[236:237] op_sel_hi:[1,0,1]\n"
    "v_pk_fma_f32 v[190:191], v[190:191], v[94:95], v[238:239] op_sel_hi:[1,0,1]\n"
    "v_rcp_f32 v64, v64\n"
    "v_exp_f32 v76, v184\n"
    "v_rcp_f32 v65, v65\n"
    "v_exp_f32 v77, v185\n"
    "v_rcp_f32 v66, v66\n"
    "v_exp_f32 v78, v186\n"
    "v_rcp_f32 v67, v67\n"
    "v_exp_f32 v79, v187\n"
    "v_rcp_f32 v68, v68\n"
    "v_exp_f32 v80, v188\n"
    "v_rcp_f32 v69, v69\n"
    "v_exp_f32 v81, v189\n"
    "v_rcp_f32 v70, v70\n"
    "v_exp_f32 v82, v190\n"
    "v_rcp_f32 v71, v71\n"
    "v_exp_f32 v83, v191\n"
    "v_pk_add_f32 v[64:65], v[64:65], s[22:23] op_sel_hi:[1,0] neg_lo:[1,0] neg_hi:[1,0]\n"
    "v_pk_add_f32 v[66:67], v[66:67], s[22:23] op_sel_hi:[1,0] neg_lo:[1,0] neg_hi:[1,0]\n"
    "v_pk_add_f32 v[68:69], v[68:69], s[22:23] op_sel_hi:[1,0] neg_lo:[1,0] neg_hi:[1,0]\n"
    "v_pk_add_f32 v[70:71], v[70:71], s[22:23] op_sel_hi:[1,0] neg_lo:[1,0] neg_hi:[1,0]\n"
    "v_pk_mul_f32 v[176:177], v[176:177], v[64:65]\n"
    "v_pk_mul_f32 v[178:179], v[178:179], v[66:67]\n"
    "v_pk_mul_f32 v[180:181], v[180:181], v[68:69]\n"
    "v_pk_mul_f32 v[182:183], v[182:183], v[70:71]\n"
    "v_pk_fma_f32 v[76:77], v[76:77], s[20:21], s[20:21] op_sel_hi:[1,0,0]\n"
    "v_pk_fma_f32 v[78:79], v[78:79], s[20:21], s[20:21] op_sel_hi:[1,0,0]\n"
    "v_pk_fma_f32 v[80:81], v[80:81], s[20:21], s[20:21] op_sel_hi:[1,0,0]\n"
    "v_pk_fma_f32 v[82:83], v[82:83], s[20:21], s[20:21] op_sel_hi:[1,0,0]\n"
    "v_pk_fma_f32 v[76:77], v[76:77], v[76:77], s[20:21] op_sel:[0,0,1]\n"
    "v_pk_fma_f32 v[78:79], v[78:79], v[78:79], s[20:21] op_sel:[0,0,1]\n"
    "v_pk_fma_f32 v[80:81], v[80:81], v[80:81], s[20:21] op_sel:[0,0,1]\n"
    "v_pk_fma_f32 v[82:83], v[82:83], v[82:83], s[20:21] op_sel:[0,0,1]\n"
    "v_cvt_pk_bf16_f32 v72, v176, v177\n"
    "v_cvt_pk_bf16_f32 v73, v178, v179\n"
    "v_cvt_pk_bf16_f32 v74, v180, v181\n"
    "v_cvt_pk_bf16_f32 v75, v182, v183\n"
    "buffer_store_dwordx4 v[72:75], v247, s[52:55], s13 offen offset:1024 sc1\n"
    "v_rcp_f32 v76, v76\n"
    "v_rcp_f32 v77, v77\n"
    "v_rcp_f32 v78, v78\n"
    "v_rcp_f32 v79, v79\n"
    "v_rcp_f32 v80, v80\n"
    "v_rcp_f32 v81, v81\n"
    "v_rcp_f32 v82, v82\n"
    "v_rcp_f32 v83, v83\n"
    "v_pk_add_f32 v[76:77], v[76:77], s[22:23] op_sel_hi:[1,0] neg_lo:[1,0] neg_hi:[1,0]\n"
    "v_pk_add_f32 v[78:79], v[78:79], s[22:23] op_sel_hi:[1,0] neg_lo:[1,0] neg_hi:[1,0]\n"
    "v_pk_add_f32 v[80:81], v[80:81], s[22:23] op_sel_hi:[1,0] neg_lo:[1,0] neg_hi:[1,0]\n"
    "v_pk_add_f32 v[82:83], v[82:83], s[22:23] op_sel_hi:[1,0] neg_lo:[1,0] neg_hi:[1,0]\n"
    "v_pk_mul_f32 v[184:185], v[184:185], v[76:77]\n"
    "v_pk_mul_f32 v[186:187], v[186:187], v[78:79]\n"
    "v_pk_mul_f32 v[188:189], v[188:189], v[80:81]\n"
    "v_pk_mul_f32 v[190:191], v[190:191], v[82:83]\n"
    "v_cvt_pk_bf16_f32 v240, v184, v185\n"
    "v_cvt_pk_bf16_f32 v241, v186, v187\n"
    "v_cvt_pk_bf16_f32 v242, v188, v189\n"
    "v_cvt_pk_bf16_f32 v243, v190, v191\n"
    "buffer_store_dwordx4 v[240:243], v247, s[52:55], s13 offen offset:1280 sc1\n"
    ".Lo15_%=:\n"
    "ds_read_b64_tr_b16 a[48:49], v10\n"
    "ds_read_b64_tr_b16 a[50:51], v10 offset:1024\n"
    "ds_read_b64_tr_b16 a[52:53], v11\n"
    "ds_read_b64_tr_b16 a[54:55], v11 offset:1024\n"
    "ds_read_b64_tr_b16 a[56:57], v10 offset:2048\n"
    "ds_read_b64_tr_b16 a[58:59], v10 offset:3072\n"
    "ds_read_b64_tr_b16 a[60:61], v11 offset:2048\n"
    "ds_read_b64_tr_b16 a[62:63], v11 offset:3072\n"
    "ds_read_b64_tr_b16 a[64:65], v10 offset:4096\n"
    "ds_read_b64_tr_b16 a[66:67], v10 offset:5120\n"
    "ds_read_b64_tr_b16 a[68:69], v11 offset:4096\n"
    "ds_read_b64_tr_b16 a[70:71], v11 offset:5120\n"
    "ds_read_b64_tr_b16 a[72:73], v10 offset:6144\n"
    "ds_read_b64_tr_b16 a[74:75], v10 offset:7168\n"
    "ds_read_b64_tr_b16 a[76:77], v11 offset:6144\n"
    "ds_read_b64_tr_b16 a[78:79], v11 offset:7168\n"
    "ds_read_b64_tr_b16 a[80:81], v10 offset:8192\n"
    "ds_read_b64_tr_b16 a[82:83], v10 offset:9216\n"
    "ds_read_b64_tr_b16 a[84:85], v11 offset:8192\n"
    "ds_read_b64_tr_b16 a[86:87], v11 offset:9216\n"
    "ds_read_b64_tr_b16 a[88:89], v10 offset:10240\n"
    "ds_read_b64_tr_b16 a[90:91], v10 offset:11264\n"
    "ds_read_b64_tr_b16 a[92:93], v11 offset:10240\n"
    "ds_read_b64_tr_b16 a[94:95], v11 offset:11264\n"
    "s_lshl_b32 s75, s59, 3\n"
    "s_lshr_b32 s76, 0xc080400, s75\n"
    "s_and_b32 s76, s76, 0xff\n"
    "s_lshr_b32 s77, 0xf0c0803, s75\n"
    "s_and_b32 s77, s77, 0xff\n"
    "v_and_b32 v254, 7, v23\n"
    "v_add_u32 v254, s76, v254\n"
    "v_min_u32 v254, s77, v254\n"
    "s_lshl_b32 s75, s60, 9\n"
    "v_lshl_add_u32 v254, v254, 10, s75\n"
    "v_bfe_u32 v255, v23, 3, 1\n"
    "v_lshl_add_u32 v254, v255, 8, v254\n"
    "s_waitcnt vmcnt(0)\n"
    "s_mov_b32 exec_lo, 0xffffffff\n"
    "s_mov_b32 exec_hi, 0x0\n"
    "s_add_u32 s65, s12, 0x80000\n"
    "buffer_store_dword v60, v253, s[52:55], s65 offen sc1\n"
    "s_mov_b64 exec, -1\n"
    "s_waitcnt lgkmcnt(0)\n"
    "s_barrier\n"
    "s_add_u32 m0, s82, 0\n"
    "s_add_u32 s66, s81, 0x30000\n"
    "buffer_load_dwordx4 v248, s[44:47], s66 offen lds\n"
    "s_addk_i32 s66, 7168\n"
    "buffer_load_dwordx4 v248, s[44:47], s66 offen offset:1024 lds\n"
    "s_addk_i32 s66, 7168\n"
    "buffer_load_dwordx4 v248, s[44:47], s66 offen offset:2048 lds\n"
    "s_addk_i32 s66, 7168\n"
    "buffer_load_dwordx4 v248, s[44:47], s66 offen offset:3072 lds\n"
    "s_addk_i32 m0, 4096\n"
    "s_addk_i32 s66, 11264\n"
    "buffer_load_dwordx4 v248, s[44:47], s66 offen lds\n"
    "s_addk_i32 s66, 7168\n"
    "buffer_load_dwordx4 v248, s[44:47], s66 offen offset:1024 lds\n"
    "s_addk_i32 s66, 7168\n"
    "buffer_load_dwordx4 v248, s[44:47], s66 offen offset:2048 lds\n"
    "s_addk_i32 s66, 7168\n"
    "buffer_load_dwordx4 v248, s[44:47], s66 offen offset:3072 lds\n"
    "s_addk_i32 m0, 4096\n"
    "s_addk_i32 s66, 11264\n"
    "buffer_load_dwordx4 v248, s[44:47], s66 offen lds\n"
    "s_addk_i32 s66, 7168\n"
    "buffer_load_dwordx4 v248, s[44:47], s66 offen offset:1024 lds\n"
    "s_addk_i32 s66, 7168\n"
    "buffer_load_dwordx4 v248, s[44:47], s66 offen offset:2048 lds\n"
    "s_addk_i32 s66, 7168\n"
    "buffer_load_dwordx4 v248, s[44:47], s66 offen offset:3072 lds\n"
    "s_lshl_b32 s75, s60, 19\n"
    "s_lshl2_add_u32 s75, s18, s75\n"
    "s_mul_i32 s76, s59, 0x1600\n"
    "s_add_u32 s75, s75, s76\n"
    "s_add_u32 s85, s75, 0x1800000\n"
    "s_cmp_eq_u32 s59, 3\n"
    "s_cselect_b32 s75, 0x200, 0\n"
    "s_sub_u32 s86, s85, s75\n"
    "s_add_u32 s68, s74, 0x80000\n"
    "buffer_load_dword v255, v254, s[52:55], s68 offen sc1\n"
    "s_add_u32 m0, s90, 0\n"
    "s_addk_i32 s66, 11264\n"
    "buffer_load_dwordx4 v248, s[44:47], s66 offen lds\n"
    "s_addk_i32 s66, 7168\n"
    "buffer_load_dwordx4 v248, s[44:47], s66 offen offset:1024 lds\n"
    "s_addk_i32 s66, 7168\n"
    "buffer_load_dwordx4 v248, s[44:47], s66 offen offset:2048 lds\n"
    "s_addk_i32 s66, 7168\n"
    "buffer_load_dwordx4 v248, s[44:47], s66 offen offset:3072 lds\n"
    "s_addk_i32 m0, 4096\n"
    "s_addk_i32 s66, 11264\n"
    "buffer_load_dwordx4 v248, s[44:47], s66 offen lds\n"
    "s_addk_i32 s66, 7168\n"
    "buffer_load_dwordx4 v248, s[44:47], s66 offen offset:1024 lds\n"
    "s_addk_i32 s66, 7168\n"
    "buffer_load_dwordx4 v248, s[44:47], s66 offen offset:2048 lds\n"
    "s_addk_i32 s66, 7168\n"
    "buffer_load_dwordx4 v248, s[44:47], s66 offen offset:3072 lds\n"
    "s_barrier\n"
    "s_mov_b32 s69, 0\n"
    "s_waitcnt vmcnt(8)\n"
    "v_cmp_ne_u32 vcc, s62, v255\n"
    "s_nop 0\n"
    "s_cmp_eq_u64 vcc, 0\n"
    "s_cbranch_scc1 .Ld18_%=\n"
    ".Lp16_%=:\n"
    "s_add_u32 s68, s74, 0x80000\n"
    "buffer_load_dword v255, v254, s[52:55], s68 offen sc1\n"
    "s_waitcnt vmcnt(0)\n"
    "v_cmp_ne_u32 vcc, s62, v255\n"
    "s_nop 0\n"
    "s_cmp_eq_u64 vcc, 0\n"
    "s_cbranch_scc1 .Ld18_%=\n"
    "s_add_u32 s69, s69, 1\n"
    "s_cmp_lt_u32 s69, 0x20000\n"
    "s_cbranch_scc1 .Lp16_%=\n"
    "s_nop 7\n"
    ".Ld18_%=:\n"
    "buffer_load_dwordx4 a[176:179], v247, s[52:55], s85 offen sc1\n"
    "s_add_u32 s67, s85, 0x40000\n"
    "buffer_load_dwordx4 a[180:183], v247, s[52:55], s67 offen sc1\n"
    "buffer_load_dwordx4 a[184:187], v247, s[52:55], s85 offen offset:256 sc1\n"
    "buffer_load_dwordx4 a[188:191], v247, s[52:55], s67 offen offset:256 sc1\n"
    "buffer_load_dwordx4 a[192:195], v247, s[52:55], s85 offen offset:512 sc1\n"
    "buffer_load_dwordx4 a[196:199], v247, s[52:55], s67 offen offset:512 sc1\n"
    "buffer_load_dwordx4 a[200:203], v247, s[52:55], s85 offen offset:768 sc1\n"
    "buffer_load_dwordx4 a[204:207], v247, s[52:55], s67 offen offset:768 sc1\n"
    "buffer_load_dwordx4 a[208:211], v247, s[52:55], s85 offen offset:1024 sc1\n"
    "buffer_load_dwordx4 a[212:215], v247, s[52:55], s67 offen offset:1024 sc1\n"
    "v_and_b32 v61, 31, v23\n"
    "v_lshrrev_b32 v62, 5, v23\n"
    "v_xor_b32 v62, v62, v21\n"
    "v_lshlrev_b32 v62, 4, v62\n"
    "v_lshl_add_u32 v61, v61, 8, v62\n"
    "s_lshl_b32 s75, s59, 14\n"
    "s_add_u32 s75, s75, 0x18000\n"
    "v_add_u32 v224, s75, v61\n"
    "v_xor_b32 v230, 0, v224\n"
    "v_xor_b32 v231, 32, v224\n"
    "v_xor_b32 v232, 64, v224\n"
    "v_xor_b32 v233, 0x60, v224\n"
    "v_xor_b32 v234, 0x80, v224\n"
    "v_xor_b32 v235, 0xa0, v224\n"
    "v_xor_b32 v236, 0xc0, v224\n"
    "v_xor_b32 v237, 0xe0, v224\n"
    "v_xor_b32 v61, v22, v21\n"
    "v_lshlrev_b32 v61, 4, v61\n"
    "v_lshl_add_u32 v61, v21, 8, v61\n"
    "s_lshl_b32 s75, s59, 12\n"
    "s_add_u32 s76, s75, 0x14000\n"
    "s_lshl_b32 s75, s59, 11\n"
    "s_add_u32 s75, s75, 0x18000\n"
    "v_add_u32 v225, s76, v61\n"
    "v_lshrrev_b32 v61, 3, v23\n"
    "v_and_b32 v62, 7, v23\n"
    "v_lshlrev_b32 v228, 4, v62\n"
    "v_lshl_add_u32 v228, v61, 10, v228\n"
    "v_lshlrev_b32 v63, 1, v62\n"
    "v_xor_b32 v63, v63, v61\n"
    "v_lshlrev_b32 v63, 4, v63\n"
    "v_lshl_add_u32 v63, v61, 8, v63\n"
    "s_and_b32 s77, s59, 1\n"
    "s_lshl_b32 s77, s77, 7\n"
    "v_xor_b32 v63, s77, v63\n"
    "v_add_u32 v226, s75, v63\n"
    "v_xor_b32 v227, 16, v226\n"
    "v_subrev_u32 v238, 0x4000, v226\n"
    "v_subrev_u32 v239, 0x4000, v227\n"
    "s_lshl_b32 s83, s87, 10\n"
    "s_lshl_b32 s75, s61, 7\n"
    "s_add_u32 s83, s83, s75\n"
    "s_lshl_b32 s75, s59, 13\n"
    "s_sub_u32 s83, s83, s75\n"
    "s_waitcnt vmcnt(9)\n"
    "v_mfma_f32_32x32x16_bf16 v[160:175], a[0:3], a[176:179], 0\n"
    "buffer_load_dwordx4 a[216:219], v247, s[52:55], s85 offen offset:1280 sc1\n"
    "v_mfma_f32_32x32x16_bf16 v[176:191], a[4:7], a[176:179], 0\n"
    "ds_read_b64_tr_b16 a[96:97], v10\n"
    "ds_read_b64_tr_b16 a[98:99], v10 offset:1024\n"
    "s_waitcnt vmcnt(9)\n"
    "v_mfma_f32_32x32x16_bf16 v[192:207], a[0:3], a[180:183], 0\n"
    "buffer_load_dwordx4 a[220:223], v247, s[52:55], s67 offen offset:1280 sc1\n"
    "v_mfma_f32_32x32x16_bf16 v[208:223], a[4:7], a[180:183], 0\n"
    "ds_read_b64_tr_b16 a[100:101], v11\n"
    "ds_read_b64_tr_b16 a[102:103], v11 offset:1024\n"
    "s_waitcnt vmcnt(9)\n"
    "v_mfma_f32_32x32x16_bf16 v[160:175], a[8:11], a[184:187], v[160:175]\n"
    "buffer_load_dwordx4 a[224:227], v247, s[52:55], s85 offen offset:1536 sc1\n"
    "v_mfma_f32_32x32x16_bf16 v[176:191], a[12:15], a[184:187], v[176:191]\n"
    "ds_read_b64_tr_b16 a[104:105], v10 offset:2048\n"
    "ds_read_b64_tr_b16 a[106:107], v10 offset:3072\n"
    "s_waitcnt vmcnt(9)\n"
    "v_mfma_f32_32x32x16_bf16 v[192:207], a[8:11], a[188:191], v[192:207]\n"
    "buffer_load_dwordx4 a[228:231], v247, s[52:55], s67 offen offset:1536 sc1\n"
    "v_mfma_f32_32x32x16_bf16 v[208:223], a[12:15], a[188:191], v[208:223]\n"
    "ds_read_b64_tr_b16 a[108:109], v11 offset:2048\n"
    "ds_read_b64_tr_b16 a[110:111], v11 offset:3072\n"
    "s_waitcnt vmcnt(9)\n"
    "v_mfma_f32_32x32x16_bf16 v[160:175], a[16:19], a[192:195], v[160:175]\n"
    "buffer_load_dwordx4 a[232:235], v247, s[52:55], s85 offen offset:1792 sc1\n"
    "v_mfma_f32_32x32x16_bf16 v[176:191], a[20:23], a[192:195], v[176:191]\n"
    "ds_read_b64_tr_b16 a[112:113], v10 offset:4096\n"
    "ds_read_b64_tr_b16 a[114:115], v10 offset:5120\n"
    "s_waitcnt vmcnt(9)\n"
    "v_mfma_f32_32x32x16_bf16 v[192:207], a[16:19], a[196:199], v[192:207]\n"
    "buffer_load_dwordx4 a[236:239], v247, s[52:55], s67 offen offset:1792 sc1\n"
    "v_mfma_f32_32x32x16_bf16 v[208:223], a[20:23], a[196:199], v[208:223]\n"
    "ds_read_b64_tr_b16 a[116:117], v11 offset:4096\n"
    "ds_read_b64_tr_b16 a[118:119], v11 offset:5120\n"
    "s_waitcnt vmcnt(9)\n"
    "v_mfma_f32_32x32x16_bf16 v[160:175], a[24:27], a[200:203], v[160:175]\n"
    "buffer_load_dwordx4 a[240:243], v247, s[52:55], s85 offen offset:2048 sc1\n"
    "v_mfma_f32_32x32x16_bf16 v[176:191], a[28:31], a[200:203], v[176:191]\n"
    "ds_read_b64_tr_b16 a[120:121], v10 offset:6144\n"
    "ds_read_b64_tr_b16 a[122:123], v10 offset:7168\n"
    "s_waitcnt vmcnt(9)\n"
    "v_mfma_f32_32x32x16_bf16 v[192:207], a[24:27], a[204:207], v[192:207]\n"
    "buffer_load_dwordx4 a[244:247], v247, s[52:55], s67 offen offset:2048 sc1\n"
    "v_mfma_f32_32x32x16_bf16 v[208:223], a[28:31], a[204:207], v[208:223]\n"
    "ds_read_b64_tr_b16 a[124:125], v11 offset:6144\n"
    "ds_read_b64_tr_b16 a[126:127], v11 offset:7168\n"
    "s_waitcnt vmcnt(9)\n"
    "v_mfma_f32_32x32x16_bf16 v[160:175], a[32:35], a[208:211], v[160:175]\n"
    "buffer_load_dwordx4 a[248:251], v247, s[52:55], s85 offen offset:2304 sc1\n"
    "v_mfma_f32_32x32x16_bf16 v[176:191], a[36:39], a[208:211], v[176:191]\n"
    "ds_read_b64_tr_b16 a[128:129], v10 offset:8192\n"
    "ds_read_b64_tr_b16 a[130:131], v10 offset:9216\n"
    "s_waitcnt vmcnt(9)\n"
    "v_mfma_f32_32x32x16_bf16 v[192:207], a[32:35], a[212:215], v[192:207]\n"
    "buffer_load_dwordx4 a[252:255], v247, s[52:55], s67 offen offset:2304 sc1\n"
    "v_mfma_f32_32x32x16_bf16 v[208:223], a[36:39], a[212:215], v[208:223]\n"
    "ds_read_b64_tr_b16 a[132:133], v11 offset:8192\n"
    "ds_read_b64_tr_b16 a[134:135], v11 offset:9216\n"
    "s_waitcnt vmcnt(9)\n"
    "v_mfma_f32_32x32x16_bf16 v[160:175], a[40:43], a[216:219], v[160:175]\n"
    "buffer_load_dwordx4 v[64:67], v247, s[52:55], s85 offen offset:2560 sc1\n"
    "v_mfma_f32_32x32x16_bf16 v[176:191], a[44:47], a[216:219], v[176:191]\n"
    "ds_read_b64_tr_b16 a[136:137], v10 offset:10240\n"
    "ds_read_b64_tr_b16 a[138:139], v10 offset:11264\n"
    "s_waitcnt vmcnt(9)\n"
    "v_mfma_f32_32x32x16_bf16 v[192:207], a[40:43], a[220:223], v[192:207]\n"
    "buffer_load_dwordx4 v[68:71], v247, s[52:55], s67 offen offset:2560 sc1\n"
    "v_mfma_f32_32x32x16_bf16 v[208:223], a[44:47], a[220:223], v[208:223]\n"
    "ds_read_b64_tr_b16 a[140:141], v11 offset:10240\n"
    "ds_read_b64_tr_b16 a[142:143], v11 offset:11264\n"
    "s_waitcnt vmcnt(9)\n"
    "v_mfma_f32_32x32x16_bf16 v[160:175], a[48:51], a[224:227], v[160:175]\n"
    "buffer_load_dwordx4 v[72:75], v247, s[52:55], s85 offen offset:2816 sc1\n"
    "v_mfma_f32_32x32x16_bf16 v[176:191], a[52:55], a[224:227], v[176:191]\n"
    "ds_read_b64_tr_b16 a[144:145], v12\n"
    "ds_read_b64_tr_b16 a[146:147], v12 offset:1024\n"
    "s_waitcnt vmcnt(9)\n"
    "v_mfma_f32_32x32x16_bf16 v[192:207], a[48:51], a[228:231], v[192:207]\n"
    "buffer_load_dwordx4 v[76:79], v247, s[52:55], s67 offen offset:2816 sc1\n"
    "v_mfma_f32_32x32x16_bf16 v[208:223], a[52:55], a[228:231], v[208:223]\n"
    "ds_read_b64_tr_b16 a[148:149], v13\n"
    "ds_read_b64_tr_b16 a[150:151], v13 offset:1024\n"
    "s_waitcnt vmcnt(9)\n"
    "v_mfma_f32_32x32x16_bf16 v[160:175], a[56:59], a[232:235], v[160:175]\n"
    "buffer_load_dwordx4 v[80:83], v247, s[52:55], s85 offen offset:3072 sc1\n"
    "v_mfma_f32_32x32x16_bf16 v[176:191], a[60:63], a[232:235], v[176:191]\n"
    "ds_read_b64_tr_b16 a[152:153], v12 offset:2048\n"
    "ds_read_b64_tr_b16 a[154:155], v12 offset:3072\n"
    "s_waitcnt vmcnt(9)\n"
    "v_mfma_f32_32x32x16_bf16 v[192:207], a[56:59], a[236:239], v[192:207]\n"
    "buffer_load_dwordx4 v[84:87], v247, s[52:55], s67 offen offset:3072 sc1\n"
    "v_mfma_f32_32x32x16_bf16 v[208:223], a[60:63], a[236:239], v[208:223]\n"
    "ds_read_b64_tr_b16 a[156:157], v13 offset:2048\n"
    "ds_read_b64_tr_b16 a[158:159], v13 offset:3072\n"
    "s_waitcnt vmcnt(9)\n"
    "v_mfma_f32_32x32x16_bf16 v[160:175], a[64:67], a[240:243], v[160:175]\n"
    "buffer_load_dwordx4 v[88:91], v247, s[52:55], s85 offen offset:3328 sc1\n"
    "v_mfma_f32_32x32x16_bf16 v[176:191], a[68:71], a[240:243], v[176:191]\n"
    "ds_read_b64_tr_b16 a[160:161], v12 offset:4096\n"
    "ds_read_b64_tr_b16 a[162:163], v12 offset:5120\n"
    "s_waitcnt vmcnt(9)\n"
    "v_mfma_f32_32x32x16_bf16 v[192:207], a[64:67], a[244:247], v[192:207]\n"
    "buffer_load_dwordx4 v[92:95], v247, s[52:55], s67 offen offset:3328 sc1\n"
    "v_mfma_f32_32x32x16_bf16 v[208:223], a[68:71], a[244:247], v[208:223]\n"
    "ds_read_b64_tr_b16 a[164:165], v13 offset:4096\n"
    "ds_read_b64_tr_b16 a[166:167], v13 offset:5120\n"
    "s_waitcnt vmcnt(9)\n"
    "v_mfma_f32_32x32x16_bf16 v[160:175], a[72:75], a[248:251], v[160:175]\n"
    "buffer_load_dwordx4 v[96:99], v247, s[52:55], s85 offen offset:3584 sc1\n"
    "v_mfma_f32_32x32x16_bf16 v[176:191], a[76:79], a[248:251], v[176:191]\n"
    "ds_read_b64_tr_b16 a[168:169], v12 offset:6144\n"
    "ds_read_b64_tr_b16 a[170:171], v12 offset:7168\n"
    "s_waitcnt vmcnt(9)\n"
    "v_mfma_f32_32x32x16_bf16 v[192:207], a[72:75], a[252:255], v[192:207]\n"
    "buffer_load_dwordx4 v[100:103], v247, s[52:55], s67 offen offset:3584 sc1\n"
    "v_mfma_f32_32x32x16_bf16 v[208:223], a[76:79], a[252:255], v[208:223]\n"
    "ds_read_b64_tr_b16 a[172:173], v13 offset:6144\n"
    "ds_read_b64_tr_b16 a[174:175], v13 offset:7168\n"
    "s_waitcnt vmcnt(9)\n"
    "v_mfma_f32_32x32x16_bf16 v[160:175], a[80:83], v[64:67], v[160:175]\n"
    "buffer_load_dwordx4 v[104:107], v247, s[52:55], s85 offen offset:3840 sc1\n"
    "v_mfma_f32_32x32x16_bf16 v[176:191], a[84:87], v[64:67], v[176:191]\n"
    "ds_write_b128 v225, v[28:31]\n"
    "s_waitcnt vmcnt(9)\n"
    "v_mfma_f32_32x32x16_bf16 v[192:207], a[80:83], v[68:71], v[192:207]\n"
    "buffer_load_dwordx4 v[108:111], v247, s[52:55], s67 offen offset:3840 sc1\n"
    "v_mfma_f32_32x32x16_bf16 v[208:223], a[84:87], v[68:71], v[208:223]\n"
    "v_xor_b32 v61, 64, v225\n"
    "ds_write_b128 v61, v[32:35]\n"
    "s_waitcnt vmcnt(9)\n"
    "v_mfma_f32_32x32x16_bf16 v[160:175], a[88:91], v[72:75], v[160:175]\n"
    "s_add_u32 s65, s85, 0x1000\n"
    "buffer_load_dwordx4 v[112:115], v247, s[52:55], s65 offen sc1\n"
    "v_mfma_f32_32x32x16_bf16 v[176:191], a[92:95], v[72:75], v[176:191]\n"
    "v_xor_b32 v61, 0x80, v225\n"
    "ds_write_b128 v61, v[36:39]\n"
    "s_waitcnt vmcnt(9)\n"
    "v_mfma_f32_32x32x16_bf16 v[192:207], a[88:91], v[76:79], v[192:207]\n"
    "buffer_load_dwordx4 v[120:123], v247, s[52:55], s65 offen offset:256 sc1\n"
    "v_mfma_f32_32x32x16_bf16 v[208:223], a[92:95], v[76:79], v[208:223]\n"
    "v_xor_b32 v61, 0xc0, v225\n"
    "ds_write_b128 v61, v[40:43]\n"
    "s_waitcnt vmcnt(9)\n"
    "s_waitcnt lgkmcnt(15)\n"
    "v_mfma_f32_32x32x16_bf16 v[160:175], a[96:99], v[80:83], v[160:175]\n"
    "buffer_load_dwordx4 v[128:131], v247, s[52:55], s65 offen offset:512 sc1\n"
    "v_mfma_f32_32x32x16_bf16 v[176:191], a[100:103], v[80:83], v[176:191]\n"
    "s_waitcnt vmcnt(9)\n"
    "v_mfma_f32_32x32x16_bf16 v[192:207], a[96:99], v[84:87], v[192:207]\n"
    "buffer_load_dwordx4 v[136:139], v247, s[52:55], s65 offen offset:768 sc1\n"
    "v_mfma_f32_32x32x16_bf16 v[208:223], a[100:103], v[84:87], v[208:223]\n"
    "s_waitcnt vmcnt(9)\n"
    "v_mfma_f32_32x32x16_bf16 v[160:175], a[104:107], v[88:91], v[160:175]\n"
    "s_add_u32 s65, s86, 0x1400\n"
    "buffer_load_dwordx4 v[144:147], v247, s[52:55], s65 offen sc1\n"
    "v_mfma_f32_32x32x16_bf16 v[176:191], a[108:111], v[88:91], v[176:191]\n"
    "s_waitcnt vmcnt(9)\n"
    "v_mfma_f32_32x32x16_bf16 v[192:207], a[104:107], v[92:95], v[192:207]\n"
    "buffer_load_dwordx4 v[152:155], v247, s[52:55], s65 offen offset:256 sc1\n"
    "v_mfma_f32_32x32x16_bf16 v[208:223], a[108:111], v[92:95], v[208:223]\n"
    "s_waitcnt vmcnt(9)\n"
    "v_mfma_f32_32x32x16_bf16 v[160:175], a[112:115], v[96:99], v[160:175]\n"
    "s_addk_i32 s67, 4096\n"
    "buffer_load_dwordx4 v[116:119], v247, s[52:55], s67 offen sc1\n"
    "v_mfma_f32_32x32x16_bf16 v[176:191], a[116:119], v[96:99], v[176:191]\n"
    "s_waitcnt vmcnt(9)\n"
    "v_mfma_f32_32x32x16_bf16 v[192:207], a[112:115], v[100:103], v[192:207]\n"
    "buffer_load_dwordx4 v[124:127], v247, s[52:55], s67 offen offset:256 sc1\n"
    "v_mfma_f32_32x32x16_bf16 v[208:223], a[116:119], v[100:103], v[208:223]\n"
    "s_waitcnt vmcnt(9)\n"
    "v_mfma_f32_32x32x16_bf16 v[160:175], a[120:123], v[104:107], v[160:175]\n"
    "buffer_load_dwordx4 v[132:135], v247, s[52:55], s67 offen offset:512 sc1\n"
    "v_mfma_f32_32x32x16_bf16 v[176:191], a[124:127], v[104:107], v[176:191]\n"
    "s_waitcnt vmcnt(9)\n"
    "v_mfma_f32_32x32x16_bf16 v[192:207], a[120:123], v[108:111], v[192:207]\n"
    "buffer_load_dwordx4 v[140:143], v247, s[52:55], s67 offen offset:768 sc1\n"
    "v_mfma_f32_32x32x16_bf16 v[208:223], a[124:127], v[108:111], v[208:223]\n"
    "s_waitcnt vmcnt(9)\n"
    "v_mfma_f32_32x32x16_bf16 v[160:175], a[128:131], v[112:115], v[160:175]\n"
    "s_add_u32 s67, s86, 0x41400\n"
    "buffer_load_dwordx4 v[148:151], v247, s[52:55], s67 offen sc1\n"
    "v_mfma_f32_32x32x16_bf16 v[176:191], a[132:135], v[112:115], v[176:191]\n"
    "s_waitcnt vmcnt(9)\n"
    "v_mfma_f32_32x32x16_bf16 v[160:175], a[136:139], v[120:123], v[160:175]\n"
    "buffer_load_dwordx4 v[156:159], v247, s[52:55], s67 offen offset:256 sc1\n"
    "v_mfma_f32_32x32x16_bf16 v[176:191], a[140:143], v[120:123], v[176:191]\n"
    "s_waitcnt vmcnt(9)\n"
    "v_mfma_f32_32x32x16_bf16 v[160:175], a[144:147], v[128:131], v[160:175]\n"
    "v_mfma_f32_32x32x16_bf16 v[176:191], a[148:151], v[128:131], v[176:191]\n"
    "s_waitcnt vmcnt(8)\n"
    "s_waitcnt lgkmcnt(14)\n"
    "v_mfma_f32_32x32x16_bf16 v[160:175], a[152:155], v[136:139], v[160:175]\n"
    "s_waitcnt lgkmcnt(12)\n"
    "v_mfma_f32_32x32x16_bf16 v[176:191], a[156:159], v[136:139], v[176:191]\n"
    "s_waitcnt vmcnt(7)\n"
    "s_waitcnt lgkmcnt(10)\n"
    "v_mfma_f32_32x32x16_bf16 v[160:175], a[160:163], v[144:147], v[160:175]\n"
    "s_waitcnt vmcnt(6)\n"
    "s_waitcnt lgkmcnt(6)\n"
    "v_mfma_f32_32x32x16_bf16 v[160:175], a[168:171], v[152:155], v[160:175]\n"
    "v_mfma_f32_32x32x16_bf16 v[176:191], a[164:167], v[144:147], v[176:191]\n"
    "s_waitcnt lgkmcnt(4)\n"
    "v_mfma_f32_32x32x16_bf16 v[176:191], a[172:175], v[152:155], v[176:191]\n"
    "s_nop 8\n"
    "ds_write_b128 v230, v[160:163]\n"
    "ds_write_b128 v231, v[164:167]\n"
    "s_waitcnt vmcnt(5)\n"
    "v_mfma_f32_32x32x16_bf16 v[192:207], a[128:131], v[116:119], v[192:207]\n"
    "ds_write_b128 v232, v[168:171]\n"
    "ds_write_b128 v233, v[172:175]\n"
    "v_mfma_f32_32x32x16_bf16 v[208:223], a[132:135], v[116:119], v[208:223]\n"
    "ds_write_b128 v234, v[176:179]\n"
    "ds_write_b128 v235, v[180:183]\n"
    "s_waitcnt vmcnt(4)\n"
    "v_mfma_f32_32x32x16_bf16 v[192:207], a[136:139], v[124:127], v[192:207]\n"
    "ds_write_b128 v236, v[184:187]\n"
    "ds_write_b128 v237, v[188:191]\n"
    "v_mfma_f32_32x32x16_bf16 v[208:223], a[140:143], v[124:127], v[208:223]\n"
    "s_waitcnt vmcnt(3)\n"
    "v_mfma_f32_32x32x16_bf16 v[192:207], a[144:147], v[132:135], v[192:207]\n"
    "s_waitcnt lgkmcnt(0)\n"
    "s_barrier\n"
    "ds_read_b128 v[96:99], v238\n"
    "ds_read_b128 v[100:103], v239\n"
    "ds_read_b128 v[64:67], v226\n"
    "ds_read_b128 v[68:71], v227\n"
    "ds_read_b128 v[72:75], v226 offset:16384\n"
    "s_waitcnt vmcnt(2)\n"
    "v_mfma_f32_32x32x16_bf16 v[192:207], a[152:155], v[140:143], v[192:207]\n"
    "ds_read_b128 v[76:79], v227 offset:16384\n"
    "ds_read_b128 v[80:83], v226 offset:32768\n"
    "ds_read_b128 v[84:87], v227 offset:32768\n"
    "ds_read_b128 v[88:91], v226 offset:49152\n"
    "ds_read_b128 v[92:95], v227 offset:49152\n"
    "s_waitcnt vmcnt(1)\n"
    "v_mfma_f32_32x32x16_bf16 v[192:207], a[160:163], v[148:151], v[192:207]\n"
    "ds_read_b128 v[144:147], v238 offset:8192\n"
    "ds_read_b128 v[152:155], v239 offset:8192\n"
    "s_waitcnt vmcnt(0)\n"
    "v_mfma_f32_32x32x16_bf16 v[192:207], a[168:171], v[156:159], v[192:207]\n"
    "s_waitcnt lgkmcnt(7)\n"
    "v_add_f32 v64, v64, v72\n"
    "v_add_f32 v65, v65, v73\n"
    "v_add_f32 v66, v66, v74\n"
    "v_add_f32 v67, v67, v75\n"
    "s_waitcnt lgkmcnt(3)\n"
    "v_add_f32 v80, v80, v88\n"
    "v_mfma_f32_32x32x16_bf16 v[208:223], a[148:151], v[132:135], v[208:223]\n"
    "v_add_f32 v81, v81, v89\n"
    "v_add_f32 v82, v82, v90\n"
    "v_add_f32 v83, v83, v91\n"
    "v_add_f32 v68, v68, v76\n"
    "v_add_f32 v69, v69, v77\n"
    "v_mfma_f32_32x32x16_bf16 v[208:223], a[156:159], v[140:143], v[208:223]\n"
    "ds_write_b128 v230, v[192:195] offset:8192\n"
    "ds_write_b128 v231, v[196:199] offset:8192\n"
    "v_add_f32 v70, v70, v78\n"
    "v_add_f32 v71, v71, v79\n"
    "s_waitcnt lgkmcnt(4)\n"
    "v_add_f32 v84, v84, v92\n"
    "v_add_f32 v85, v85, v93\n"
    "v_add_f32 v86, v86, v94\n"
    "v_mfma_f32_32x32x16_bf16 v[208:223], a[164:167], v[148:151], v[208:223]\n"
    "ds_write_b128 v232, v[200:203] offset:8192\n"
    "ds_write_b128 v233, v[204:207] offset:8192\n"
    "v_add_f32 v87, v87, v95\n"
    "v_add_f32 v64, v64, v96\n"
    "v_add_f32 v65, v65, v97\n"
    "v_add_f32 v66, v66, v98\n"
    "v_add_f32 v67, v67, v99\n"
    "v_mfma_f32_32x32x16_bf16 v[208:223], a[172:175], v[156:159], v[208:223]\n"
    "v_add_f32 v68, v68, v100\n"
    "v_add_f32 v69, v69, v101\n"
    "v_add_f32 v70, v70, v102\n"
    "v_add_f32 v71, v71, v103\n"
    "v_add_f32 v64, v64, v80\n"
    "v_add_f32 v65, v65, v81\n"
    "v_add_f32 v66, v66, v82\n"
    "v_add_f32 v67, v67, v83\n"
    "v_add_f32 v68, v68, v84\n"
    "v_add_f32 v69, v69, v85\n"
    "v_add_f32 v70, v70, v86\n"
    "v_add_f32 v71, v71, v87\n"
    "v_cvt_pk_bf16_f32 v240, v64, v65\n"
    "ds_write_b128 v234, v[208:211] offset:8192\n"
    "ds_write_b128 v235, v[212:215] offset:8192\n"
    "ds_write_b128 v236, v[216:219] offset:8192\n"
    "ds_write_b128 v237, v[220:223] offset:8192\n"
    "v_cvt_pk_bf16_f32 v241, v66, v67\n"
    "v_cvt_pk_bf16_f32 v242, v68, v69\n"
    "v_cvt_pk_bf16_f32 v243, v70, v71\n"
    "buffer_store_dwordx4 v[240:243], v228, s[48:51], s83 offen sc1\n"
    "s_waitcnt lgkmcnt(0)\n"
    "s_barrier\n"
    "ds_read_b128 v[64:67], v226 offset:8192\n"
    "ds_read_b128 v[68:71], v227 offset:8192\n"
    "ds_read_b128 v[72:75], v226 offset:24576\n"
    "ds_read_b128 v[76:79], v227 offset:24576\n"
    "ds_read_b128 v[80:83], v226 offset:40960\n"
    "ds_read_b128 v[84:87], v227 offset:40960\n"
    "ds_read_b128 v[88:91], v226 offset:57344\n"
    "ds_read_b128 v[92:95], v227 offset:57344\n"
    "s_waitcnt lgkmcnt(5)\n"
    "v_pk_add_f32 v[64:65], v[64:65], v[72:73]\n"
    "v_pk_add_f32 v[66:67], v[66:67], v[74:75]\n"
    "s_waitcnt lgkmcnt(1)\n"
    "v_pk_add_f32 v[80:81], v[80:81], v[88:89]\n"
    "v_pk_add_f32 v[82:83], v[82:83], v[90:91]\n"
    "v_pk_add_f32 v[68:69], v[68:69], v[76:77]\n"
    "v_pk_add_f32 v[70:71], v[70:71], v[78:79]\n"
    "s_waitcnt lgkmcnt(0)\n"
    "v_pk_add_f32 v[84:85], v[84:85], v[92:93]\n"
    "v_pk_add_f32 v[86:87], v[86:87], v[94:95]\n"
    "v_pk_add_f32 v[64:65], v[64:65], v[144:145]\n"
    "v_pk_add_f32 v[66:67], v[66:67], v[146:147]\n"
    "v_pk_add_f32 v[68:69], v[68:69], v[152:153]\n"
    "v_pk_add_f32 v[70:71], v[70:71], v[154:155]\n"
    "v_pk_add_f32 v[64:65], v[64:65], v[80:81]\n"
    "v_pk_add_f32 v[66:67], v[66:67], v[82:83]\n"
    "v_pk_add_f32 v[68:69], v[68:69], v[84:85]\n"
    "v_pk_add_f32 v[70:71], v[70:71], v[86:87]\n"
    "v_cvt_pk_bf16_f32 v100, v64, v65\n"
    "v_cvt_pk_bf16_f32 v101, v66, v67\n"
    "v_cvt_pk_bf16_f32 v102, v68, v69\n"
    "v_cvt_pk_bf16_f32 v103, v70, v71\n"
    "s_add_u32 s65, s83, 0x8000\n"
    "buffer_store_dwordx4 v[100:103], v228, s[48:51], s65 offen sc1\n"
    ".Lmend_%=:\n"
    ".Lcend_%=:\n"
    : "+v"(tid)
    : "s"(__builtin_amdgcn_kernarg_segment_ptr()), "s"(blockIdx.x)
    : "v1", "v2", "v3", "v4", "v5", "v6", "v7", "v8", "v9", "v10", "v11", "v12", "v13", "v14", "v15", "v16", "v17", "v18", "v19", "v20", "v21", "v22", "v23", "v24", "v25", "v26", "v27", "v28", "v29", "v30", "v31", "v32", "v33", "v34", "v35", "v36", "v37", "v38", "v39", "v40", "v41", "v42", "v43", "v44", "v45", "v46", "v47", "v48", "v49", "v50", "v51", "v52", "v53", "v54", "v55", "v56", "v57", "v58", "v59", "v60", "v61", "v62", "v63", "v64", "v65", "v66", "v67", "v68", "v69", "v70", "v71", "v72", "v73", "v74", "v75", "v76", "v77", "v78", "v79", "v80", "v81", "v82", "v83", "v84", "v85", "v86", "v87", "v88", "v89", "v90", "v91", "v92", "v93", "v94", "v95", "v96", "v97", "v98", "v99", "v100", "v101", "v102", "v103", "v104", "v105", "v106", "v107", "v108", "v109", "v110", "v111", "v112", "v113", "v114", "v115", "v116", "v117", "v118", "v119", "v120", "v121", "v122", "v123", "v124", "v125", "v126", "v127", "v128", "v129", "v130", "v131", "v132", "v133", "v134", "v135", "v136", "v137", "v138", "v139", "v140", "v141", "v142", "v143", "v144", "v145", "v146", "v147", "v148", "v149", "v150", "v151", "v152", "v153", "v154", "v155", "v156", "v157", "v158", "v159", "v160", "v161", "v162", "v163", "v164", "v165", "v166", "v167", "v168", "v169", "v170", "v171", "v172", "v173", "v174", "v175", "v176", "v177", "v178", "v179", "v180", "v181", "v182", "v183", "v184", "v185", "v186", "v187", "v188", "v189", "v190", "v191", "v192", "v193", "v194", "v195", "v196", "v197", "v198", "v199", "v200", "v201", "v202", "v203", "v204", "v205", "v206", "v207", "v208", "v209", "v210", "v211", "v212", "v213", "v214", "v215", "v216", "v217", "v218", "v219", "v220", "v221", "v222", "v223", "v224", "v225", "v226", "v227", "v228", "v229", "v230", "v231", "v232", "v233", "v234", "v235", "v236", "v237", "v238", "v239", "v240", "v241", "v242", "v243", "v244", "v245", "v246", "v247", "v248", "v249", "v250", "v251", "v252", "v253", "v254", "v255", "a0", "a1", "a2", "a3", "a4", "a5", "a6", "a7", "a8", "a9", "a10", "a11", "a12", "a13", "a14", "a15", "a16", "a17", "a18", "a19", "a20", "a21", "a22", "a23", "a24", "a25", "a26", "a27", "a28", "a29", "a30", "a31", "a32", "a33", "a34", "a35", "a36", "a37", "a38", "a39", "a40", "a41", "a42", "a43", "a44", "a45", "a46", "a47", "a48", "a49", "a50", "a51", "a52", "a53", "a54", "a55", "a56", "a57", "a58", "a59", "a60", "a61", "a62", "a63", "a64", "a65", "a66", "a67", "a68", "a69", "a70", "a71", "a72", "a73", "a74", "a75", "a76", "a77", "a78", "a79", "a80", "a81", "a82", "a83", "a84", "a85", "a86", "a87", "a88", "a89", "a90", "a91", "a92", "a93", "a94", "a95", "a96", "a97", "a98", "a99", "a100", "a101", "a102", "a103", "a104", "a105", "a106", "a107", "a108", "a109", "a110", "a111", "a112", "a113", "a114", "a115", "a116", "a117", "a118", "a119", "a120", "a121", "a122", "a123", "a124", "a125", "a126", "a127", "a128", "a129", "a130", "a131", "a132", "a133", "a134", "a135", "a136", "a137", "a138", "a139", "a140", "a141", "a142", "a143", "a144", "a145", "a146", "a147", "a148", "a149", "a150", "a151", "a152", "a153", "a154", "a155", "a156", "a157", "a158", "a159", "a160", "a161", "a162", "a163", "a164", "a165", "a166", "a167", "a168", "a169", "a170", "a171", "a172", "a173", "a174", "a175", "a176", "a177", "a178", "a179", "a180", "a181", "a182", "a183", "a184", "a185", "a186", "a187", "a188", "a189", "a190", "a191", "a192", "a193", "a194", "a195", "a196", "a197", "a198", "a199", "a200", "a201", "a202", "a203", "a204", "a205", "a206", "a207", "a208", "a209", "a210", "a211", "a212", "a213", "a214", "a215", "a216", "a217", "a218", "a219", "a220", "a221", "a222", "a223", "a224", "a225", "a226", "a227", "a228", "a229", "a230", "a231", "a232", "a233", "a234", "a235", "a236", "a237", "a238", "a239", "a240", "a241", "a242", "a243", "a244", "a245", "a246", "a247", "a248", "a249", "a250", "a251", "a252", "a253", "a254", "a255", "s8", "s9", "s10", "s11", "s12", "s13", "s14", "s15", "s16", "s17", "s18", "s19", "s20", "s21", "s22", "s23", "s24", "s25", "s26", "s27", "s28", "s29", "s30", "s31", "s32", "s33", "s34", "s35", "s36", "s37", "s38", "s39", "s40", "s41", "s42", "s43", "s44", "s45", "s46", "s47", "s48", "s49", "s50", "s51", "s52", "s53", "s54", "s55", "s56", "s57", "s58", "s59", "s60", "s61", "s62", "s63", "s64", "s65", "s66", "s67", "s68", "s69", "s70", "s71", "s72", "s73", "s74", "s75", "s76", "s77", "s78", "s79", "s80", "s81", "s82", "s83", "s84", "s85", "s86", "s87", "s88", "s89", "s90", "s91", "s92", "s93", "s94", "s95", "s96", "s97", "s98", "s99", "vcc", "scc", "memory");
}

extern "C" void kernel_launch(void* const* d_in, const int* in_sizes, int n_in, void* d_out, int out_size, void* d_ws, size_t ws_size,
                              hipStream_t stream) {
  (void)in_sizes; (void)n_in; (void)out_size; (void)ws_size;
  (void)hipFuncSetAttribute((const void*)fused_block, hipFuncAttributeMaxDynamicSharedMemorySize, LDS_BYTES);
  fused_block<<<256, 256, LDS_BYTES, stream>>>(d_in[0], d_in[1], d_in[2], d_in[4], d_in[5], d_in[6], d_out, d_ws);
}
```
